```python
import torch

def _softmax(x, axis=-1):
    return torch.softmax(x, dim=axis)
def _gelu(x):
    return torch.nn.functional.gelu(x, approximate="tanh")
def _silu(x):
    return torch.nn.functional.silu(x)
def _sigmoid(x):
    return torch.sigmoid(x)
def _layernorm(x, axis=-1, eps=1e-5):
    m = x.mean(dim=axis, keepdim=True)
    v = x.var(dim=axis, keepdim=True, unbiased=False)
    return (x - m) / torch.sqrt(v + eps)
def _scatter_add(tgt, idx, src, axis=0):
    return tgt.clone().index_add_(axis, idx, src)
def _segment_sum(src, seg, n):
    out = torch.zeros((n,) + src.shape[1:], dtype=src.dtype)
    return out.index_add_(0, seg, src)
def _pad(x, pw):
    flat = tuple(v for pair in reversed(pw) for v in pair)
    return torch.nn.functional.pad(x, flat)

kernel_name = 'concat_heads__ssm_conv_s5__grouped_kv_mqa__prompt'

def setup_inputs():

    _w = (lambda a: (lambda t: t.to(torch.bfloat16) if t.is_floating_point() else t)(torch.as_tensor(a)))
    def _A_setup():
        B, S, D = 1, 2048, 512
        P = D
        Lambda_re = -0.5 * (lambda _x: torch.exp(torch.as_tensor(_x)))((lambda *s: torch.randn(*s, dtype=torch.bfloat16) * 0.2)(P))
        Lambda_im = (lambda *s: torch.randn(*s, dtype=torch.bfloat16) * 0.4)(P) * 1.0
        B_re = (lambda *s: torch.randn(*s, dtype=torch.bfloat16) * 0.2)(P, D) * (D ** -0.5)
        B_im = (lambda *s: torch.randn(*s, dtype=torch.bfloat16) * 0.2)(P, D) * (D ** -0.5)
        C_re = (lambda *s: torch.randn(*s, dtype=torch.bfloat16) * 0.2)(D, P) * (P ** -0.5)
        C_im = (lambda *s: torch.randn(*s, dtype=torch.bfloat16) * 0.2)(D, P) * (P ** -0.5)
        log_dt = (lambda *s: torch.randn(*s, dtype=torch.bfloat16) * 0.2)(P) * 0.3 - 2.0
        Dskip = (lambda *s: torch.randn(*s, dtype=torch.bfloat16) * 0.05)(D)
        return {
            "x": _w((lambda *s: torch.randn(*s, dtype=torch.bfloat16) * 0.05)(B, S, D)),
            "Lambda_re": _w(Lambda_re),
            "Lambda_im": _w(Lambda_im),
            "B_re": _w(B_re),
            "B_im": _w(B_im),
            "C_re": _w(C_re),
            "C_im": _w(C_im),
            "log_dt": _w(log_dt),
            "Dskip": _w(Dskip),
        }
    _A_in = _A_setup()

    def _B_setup():
        B, S, D, H = 1, 2048, 512, 32
        Dh = D // H
        return {
            "x": _w((lambda *s: torch.randn(*s, dtype=torch.bfloat16) * 0.05)(B, S, D)),
            "wq": _w((lambda *s: torch.randn(*s, dtype=torch.bfloat16) * 0.8)(D, H * Dh)),
            "wk": _w((lambda *s: torch.randn(*s, dtype=torch.bfloat16) * 0.8)(D, Dh)),
            "wv": _w((lambda *s: torch.randn(*s, dtype=torch.bfloat16) * 0.05)(D, Dh)),
            "wo": _w((lambda *s: torch.randn(*s, dtype=torch.bfloat16) * 0.05)(H * Dh, D)),
        }
    _B_in = _B_setup()

    return {
        "A_x": _A_in["x"],
        "A_Lambda_re": _A_in["Lambda_re"],
        "A_Lambda_im": _A_in["Lambda_im"],
        "A_B_re": _A_in["B_re"],
        "A_B_im": _A_in["B_im"],
        "A_C_re": _A_in["C_re"],
        "A_C_im": _A_in["C_im"],
        "A_log_dt": _A_in["log_dt"],
        "A_Dskip": _A_in["Dskip"],
        "B_x": _B_in["x"],
        "B_wq": _B_in["wq"],
        "B_wk": _B_in["wk"],
        "B_wv": _B_in["wv"],
        "B_wo": _B_in["wo"],
    }

def reference(A_x, A_Lambda_re, A_Lambda_im, A_B_re, A_B_im, A_C_re, A_C_im, A_log_dt, A_Dskip, B_x, B_wq, B_wk, B_wv, B_wo):
    def _A_ref(x, Lambda_re, Lambda_im, B_re, B_im, C_re, C_im, log_dt, Dskip):
        Bsz, L, H = x.shape
        Lam = Lambda_re + 1j * Lambda_im
        Bmat = B_re + 1j * B_im
        Cmat = C_re + 1j * C_im
        dt = torch.exp(log_dt)
        Lam_bar = torch.exp(dt * Lam)
        B_bar = ((Lam_bar - 1.0) / Lam)[:, None] * Bmat
        Bu = torch.einsum("bld,pd->blp", x + 0j, B_bar)
        h = torch.zeros_like(Bu[:, 0, :])
        hs = []
        for t in range(L):
            h = Lam_bar[None, :] * h + Bu[:, t, :]
            hs.append(h)
        hseq = torch.stack(hs, 1)
        y = 2.0 * torch.einsum("blp,dp->bld", hseq, Cmat).real + x * Dskip[None, None, :]
        return y

    def _B_ref(x, wq, wk, wv, wo):
        B, S, D = x.shape
        H = 32
        Dh = D // H
        q = (lambda x, *d: x.permute(*d))((x @ wq).reshape(B, S, H, Dh), 0, 2, 1, 3)
        k = x @ wk
        v = x @ wv
        scores = torch.einsum("bhqd,bkd->bhqk", q, k) * (Dh ** -0.5)
        p = _softmax(scores, axis=-1)
        o = torch.einsum("bhqk,bkd->bhqd", p, v)
        return (lambda x, *d: x.permute(*d))(o, 0, 2, 1, 3).reshape(B, S, H * Dh) @ wo

    a = _A_ref(A_x, A_Lambda_re, A_Lambda_im, A_B_re, A_B_im, A_C_re, A_C_im, A_log_dt, A_Dskip)
    b = _B_ref(B_x, B_wq, B_wk, B_wv, B_wo)
    return torch.concatenate([a, b], axis=-1)
```

```cpp
#include <hip/hip_runtime.h>
#include <hip/hip_bf16.h>
#include <stdint.h>

typedef float f32x2 __attribute__((ext_vector_type(2)));
typedef float f32x4 __attribute__((ext_vector_type(4)));
typedef float f32x8 __attribute__((ext_vector_type(8)));
typedef float f32x16 __attribute__((ext_vector_type(16)));
typedef __bf16 bf16x2 __attribute__((ext_vector_type(2)));
typedef __bf16 bf16x8 __attribute__((ext_vector_type(8)));
typedef __bf16 bf16x16 __attribute__((ext_vector_type(16)));
typedef short s16x4 __attribute__((ext_vector_type(4)));
typedef unsigned u32x2 __attribute__((ext_vector_type(2)));
typedef unsigned u32x4 __attribute__((ext_vector_type(4)));
typedef unsigned short u16;
typedef unsigned long long u64;
#define LAS __attribute__((address_space(3)))
#define GAS __attribute__((address_space(1)))
#define OUTA_AUX 0
#define OUTB_AUX 0
#define DEV __device__ __forceinline__
#define RLX_AGENT __ATOMIC_RELAXED, __HIP_MEMORY_SCOPE_AGENT

constexpr unsigned WS_EPOCH = 0x0;
constexpr unsigned WS_EF = 0x1000;
constexpr unsigned WS_HF = 0x2000;
constexpr unsigned WS_KVF = 0x10000;
constexpr unsigned WS_E = 0x20000;
constexpr unsigned WS_KV = 0x40000;
constexpr unsigned WS_OF = 0x100000;
constexpr unsigned WS_HT = 0x400000;
constexpr unsigned WS_SPAN = 0x800000;

constexpr unsigned L_XB = 0, L_WQ = 65536, L_WKV = 131072;
constexpr unsigned L_SA = 131072, L_SB = 0, L_SC = 32768, L_RING = 32768, L_LEFT = 106496;
constexpr unsigned L_SE = 155648;
constexpr unsigned L_SM = 159744;
constexpr unsigned L_LAM = L_SM + 256;
constexpr unsigned L_CS = L_LEFT + 16384;
constexpr unsigned L_EX = 32768;
constexpr unsigned Y_KVOK = L_SM, Y_RDYA = L_SM + 4, Y_RDYB = L_SM + 8, Y_RDYC = L_SM + 12, Y_FREEQ = L_SM + 16, Y_FREEA = L_SM + 20, Y_FREEB = L_SM + 24,
                   Y_GRP = L_SM + 28, Y_CAR = L_SM + 32, Y_HOK = L_SM + 36;

struct Params {
  const u16 *xa, *lre, *lim, *bre, *bim, *cre, *cim, *ldt, *dsk, *xb, *wq, *wk, *wv, *wo;
  float* out; char* ws;
};

DEV unsigned f2u(float f) { return __builtin_bit_cast(unsigned, f); }
DEV float u2f(unsigned u) { return __builtin_bit_cast(float, u); }
DEV float bf2f(u16 h) { return __builtin_bit_cast(float, (unsigned)h << 16); }
DEV unsigned pk2(float a, float b) { f32x2 f = {a, b}; return __builtin_bit_cast(unsigned, __builtin_convertvector(f, bf16x2)); }
DEV void dma(const void* base, unsigned voff, unsigned lds) {
  asm volatile("s_nop 0\n\tglobal_load_lds_dwordx4 %0, %1" :: "v"(voff), "s"(base), "{m0}"(lds) : "memory");
}
DEV void dma_dev(const void* base, unsigned voff, unsigned lds) {
  asm volatile("s_nop 0\n\tglobal_load_lds_dwordx4 %0, %1 sc1" :: "v"(voff), "s"(base), "{m0}"(lds) : "memory");
}
#define WAIT_VM(n) asm volatile("s_waitcnt vmcnt(" #n ")" ::: "memory")
DEV void bar() { asm volatile("s_waitcnt lgkmcnt(0)\n\ts_barrier" ::: "memory"); }
typedef LAS char* lp;
DEV lp LP(unsigned a) { lp q = (lp)(uintptr_t)a; asm("" : "+v"(q)); return q; }
#define AT(T, q, off) (*(LAS T*)((q) + (off)))
DEV bf16x8 tr2(lp q, int o0, int o1) {
  s16x4 x = __builtin_amdgcn_ds_read_tr16_b64_v4i16((LAS s16x4*)(q + o0));
  s16x4 y = __builtin_amdgcn_ds_read_tr16_b64_v4i16((LAS s16x4*)(q + o1));
  return __builtin_bit_cast(bf16x8, __builtin_shufflevector(x, y, 0, 1, 2, 3, 4, 5, 6, 7));
}
DEV f32x16 mfma32(bf16x8 a, bf16x8 b, f32x16 c) { return __builtin_amdgcn_mfma_f32_32x32x16_bf16(a, b, c, 0, 0, 0); }
DEV f32x4 mfma16(bf16x8 a, bf16x8 b, f32x4 c) { return __builtin_amdgcn_mfma_f32_16x16x32_bf16(a, b, c, 0, 0, 0); }
DEV f32x16 smf(u32x4 a, f32x8 b, f32x16 c, int idx) { return __builtin_amdgcn_smfmac_f32_32x32x32_bf16(__builtin_bit_cast(bf16x8, a), __builtin_bit_cast(bf16x16, b), c, idx, 0, 0); }
DEV float ex2(float x) { return __builtin_amdgcn_exp2f(x); }
template <unsigned A> DEV void y_add() {
  asm volatile("s_waitcnt lgkmcnt(0)\n\ts_mov_b64 exec, 1\n\tds_add_u32 %0, %1 offset:%2\n\ts_mov_b64 exec, -1" :: "v"(L_SM), "v"(1u), "n"(A - L_SM) : "memory");
}
template <unsigned A> DEV void y_wait(unsigned target) {
  unsigned v, sv, n = 1u << 22;
  asm volatile("1:\n\tds_read_b32 %0, %3 offset:%4\n\ts_waitcnt lgkmcnt(0)\n\tv_readfirstlane_b32 %1, %0\n\ts_cmp_ge_u32 %1, %5\n\ts_cbranch_scc1 2f\n\t"
               "s_sleep 1\n\ts_sub_u32 %2, %2, 1\n\ts_cbranch_scc0 1b\n2:"
               : "=&v"(v), "=&s"(sv), "+s"(n) : "v"(L_SM), "n"(A - L_SM), "s"(target) : "memory", "scc");
}
DEV u64 ld_dev64(const char* p) { return __hip_atomic_load((GAS u64*)p, RLX_AGENT); }
DEV void st_dev64(char* p, u64 v) { __hip_atomic_store((GAS u64*)p, v, RLX_AGENT); }
constexpr int SPIN = 1 << 16;
DEV void cmac(float ar, float ai, float& r, float& i, float ur, float ui) {
  float tr = fmaf(ar, r, ur), ti = fmaf(ar, i, ui); tr = fmaf(-ai, i, tr); ti = fmaf(ai, r, ti); r = tr; i = ti;
}
DEV void csq(float& r, float& i) { float t = r * r - i * i; i = 2.f * r * i; r = t; }

#define ATT_LOOP \
  ".p2align 6\n\t" \
  "1:\n\t" \
  "v_exp_f32 v64, v64\n\t" \
  "v_exp_f32 v65, v65\n\t" \
  "v_smfmac_f32_32x32x32_bf16 v[112:127], v[148:151], v[88:95], v154\n\t" \
  "s_add_u32 %[cur], %[cur], 0x2000\n\t" \
  "s_cmp_eq_u32 %[n], 1\n\t" \
  "v_exp_f32 v66, v66\n\t" \
  "v_exp_f32 v67, v67\n\t" \
  "v_exp_f32 v68, v68\n\t" \
  "v_exp_f32 v69, v69\n\t" \
  "v_mfma_f32_32x32x16_bf16 v[80:95], v[136:139], v[132:135], 0\n\t" \
  "s_waitcnt lgkmcnt(0)\n\t" \
  "ds_read_b128 v[136:139], v152 offset:2048\n\t" \
  "v_exp_f32 v70, v70\n\t" \
  "v_exp_f32 v71, v71\n\t" \
  "v_exp_f32 v72, v72\n\t" \
  "v_exp_f32 v73, v73\n\t" \
  "v_smfmac_f32_32x32x32_bf16 v[96:111], v[140:143], v[64:71], v154\n\t" \
  "s_cselect_b32 %[cur], %[jmp], %[cur]\n\t" \
  "s_sub_u32 %[n], %[n], 1\n\t" \
  "v_exp_f32 v74, v74\n\t" \
  "v_exp_f32 v75, v75\n\t" \
  "v_exp_f32 v76, v76\n\t" \
  "v_exp_f32 v77, v77\n\t" \
  "v_exp_f32 v78, v78\n\t" \
  "v_exp_f32 v79, v79\n\t" \
  "v_exp_f32 v80, v80\n\t" \
  "v_exp_f32 v81, v81\n\t" \
  "v_smfmac_f32_32x32x32_bf16 v[96:111], v[144:147], v[72:79], v154\n\t" \
  "s_waitcnt lgkmcnt(0)\n\t" \
  "v_exp_f32 v82, v82\n\t" \
  "v_exp_f32 v83, v83\n\t" \
  "v_exp_f32 v84, v84\n\t" \
  "v_exp_f32 v85, v85\n\t" \
  "v_mfma_f32_32x32x16_bf16 v[64:79], v[136:139], v[128:131], 0\n\t" \
  "v_exp_f32 v86, v86\n\t" \
  "v_exp_f32 v87, v87\n\t" \
  "v_exp_f32 v88, v88\n\t" \
  "v_exp_f32 v89, v89\n\t" \
  "v_smfmac_f32_32x32x32_bf16 v[112:127], v[140:143], v[80:87], v154\n\t" \
  "s_mov_b64 exec, %[msk]\n\t" \
  "ds_read_b128 v[140:143], v152 offset:3072\n\t" \
  "ds_read_b128 v[148:151], v152 offset:3328\n\t" \
  "s_mov_b64 exec, -1\n\t" \
  "v_exp_f32 v90, v90\n\t" \
  "v_exp_f32 v91, v91\n\t" \
  "v_exp_f32 v92, v92\n\t" \
  "v_exp_f32 v93, v93\n\t" \
  "v_exp_f32 v94, v94\n\t" \
  "v_exp_f32 v95, v95\n\t" \
  "v_add_u32 v153, %[cur], v155\n\t" \
  "v_exp_f32 v64, v64\n\t" \
  "v_exp_f32 v65, v65\n\t" \
  "v_smfmac_f32_32x32x32_bf16 v[112:127], v[144:147], v[88:95], v154\n\t" \
  "v_exp_f32 v66, v66\n\t" \
  "v_exp_f32 v67, v67\n\t" \
  "v_exp_f32 v68, v68\n\t" \
  "v_exp_f32 v69, v69\n\t" \
  "v_mfma_f32_32x32x16_bf16 v[80:95], v[136:139], v[132:135], 0\n\t" \
  "s_waitcnt lgkmcnt(0)\n\t" \
  "ds_read_b128 v[136:139], v152 offset:4096\n\t" \
  "v_exp_f32 v70, v70\n\t" \
  "v_exp_f32 v71, v71\n\t" \
  "v_exp_f32 v72, v72\n\t" \
  "v_exp_f32 v73, v73\n\t" \
  "v_smfmac_f32_32x32x32_bf16 v[96:111], v[140:143], v[64:71], v154\n\t" \
  "v_exp_f32 v74, v74\n\t" \
  "v_exp_f32 v75, v75\n\t" \
  "v_exp_f32 v76, v76\n\t" \
  "v_exp_f32 v77, v77\n\t" \
  "v_exp_f32 v78, v78\n\t" \
  "v_exp_f32 v79, v79\n\t" \
  "v_exp_f32 v80, v80\n\t" \
  "v_exp_f32 v81, v81\n\t" \
  "v_smfmac_f32_32x32x32_bf16 v[96:111], v[148:151], v[72:79], v154\n\t" \
  "s_waitcnt lgkmcnt(0)\n\t" \
  "v_exp_f32 v82, v82\n\t" \
  "v_exp_f32 v83, v83\n\t" \
  "v_exp_f32 v84, v84\n\t" \
  "v_exp_f32 v85, v85\n\t" \
  "v_mfma_f32_32x32x16_bf16 v[64:79], v[136:139], v[128:131], 0\n\t" \
  "v_exp_f32 v86, v86\n\t" \
  "v_exp_f32 v87, v87\n\t" \
  "v_exp_f32 v88, v88\n\t" \
  "v_exp_f32 v89, v89\n\t" \
  "v_smfmac_f32_32x32x32_bf16 v[112:127], v[140:143], v[80:87], v154\n\t" \
  "s_mov_b64 exec, %[msk]\n\t" \
  "ds_read_b128 v[140:143], v152 offset:5120\n\t" \
  "ds_read_b128 v[144:147], v152 offset:5376\n\t" \
  "s_mov_b64 exec, -1\n\t" \
  "v_exp_f32 v90, v90\n\t" \
  "v_exp_f32 v91, v91\n\t" \
  "v_exp_f32 v92, v92\n\t" \
  "v_exp_f32 v93, v93\n\t" \
  "v_exp_f32 v94, v94\n\t" \
  "v_exp_f32 v95, v95\n\t" \
  "v_exp_f32 v64, v64\n\t" \
  "v_exp_f32 v65, v65\n\t" \
  "v_smfmac_f32_32x32x32_bf16 v[112:127], v[148:151], v[88:95], v154\n\t" \
  "v_exp_f32 v66, v66\n\t" \
  "v_exp_f32 v67, v67\n\t" \
  "v_exp_f32 v68, v68\n\t" \
  "v_exp_f32 v69, v69\n\t" \
  "v_mfma_f32_32x32x16_bf16 v[80:95], v[136:139], v[132:135], 0\n\t" \
  "s_waitcnt lgkmcnt(0)\n\t" \
  "ds_read_b128 v[136:139], v152 offset:6144\n\t" \
  "v_exp_f32 v70, v70\n\t" \
  "v_exp_f32 v71, v71\n\t" \
  "v_exp_f32 v72, v72\n\t" \
  "v_exp_f32 v73, v73\n\t" \
  "v_smfmac_f32_32x32x32_bf16 v[96:111], v[140:143], v[64:71], v154\n\t" \
  "v_exp_f32 v74, v74\n\t" \
  "v_exp_f32 v75, v75\n\t" \
  "v_exp_f32 v76, v76\n\t" \
  "v_exp_f32 v77, v77\n\t" \
  "v_exp_f32 v78, v78\n\t" \
  "v_exp_f32 v79, v79\n\t" \
  "v_exp_f32 v80, v80\n\t" \
  "v_exp_f32 v81, v81\n\t" \
  "v_smfmac_f32_32x32x32_bf16 v[96:111], v[144:147], v[72:79], v154\n\t" \
  "s_waitcnt lgkmcnt(0)\n\t" \
  "v_exp_f32 v82, v82\n\t" \
  "v_exp_f32 v83, v83\n\t" \
  "v_exp_f32 v84, v84\n\t" \
  "v_exp_f32 v85, v85\n\t" \
  "v_mfma_f32_32x32x16_bf16 v[64:79], v[136:139], v[128:131], 0\n\t" \
  "v_exp_f32 v86, v86\n\t" \
  "v_exp_f32 v87, v87\n\t" \
  "v_exp_f32 v88, v88\n\t" \
  "v_exp_f32 v89, v89\n\t" \
  "v_smfmac_f32_32x32x32_bf16 v[112:127], v[140:143], v[80:87], v154\n\t" \
  "s_mov_b64 exec, %[msk]\n\t" \
  "ds_read_b128 v[140:143], v152 offset:7168\n\t" \
  "ds_read_b128 v[148:151], v152 offset:7424\n\t" \
  "s_mov_b64 exec, -1\n\t" \
  "v_exp_f32 v90, v90\n\t" \
  "v_exp_f32 v91, v91\n\t" \
  "v_exp_f32 v92, v92\n\t" \
  "v_exp_f32 v93, v93\n\t" \
  "v_exp_f32 v94, v94\n\t" \
  "v_exp_f32 v95, v95\n\t" \
  "v_exp_f32 v64, v64\n\t" \
  "v_exp_f32 v65, v65\n\t" \
  "v_smfmac_f32_32x32x32_bf16 v[112:127], v[144:147], v[88:95], v154\n\t" \
  "s_cmp_lg_u32 %[n], 0\n\t" \
  "v_exp_f32 v66, v66\n\t" \
  "v_exp_f32 v67, v67\n\t" \
  "v_exp_f32 v68, v68\n\t" \
  "v_exp_f32 v69, v69\n\t" \
  "v_mfma_f32_32x32x16_bf16 v[80:95], v[136:139], v[132:135], 0\n\t" \
  "s_waitcnt lgkmcnt(0)\n\t" \
  "ds_read_b128 v[136:139], v153 offset:0\n\t" \
  "v_exp_f32 v70, v70\n\t" \
  "v_exp_f32 v71, v71\n\t" \
  "v_exp_f32 v72, v72\n\t" \
  "v_exp_f32 v73, v73\n\t" \
  "v_smfmac_f32_32x32x32_bf16 v[96:111], v[140:143], v[64:71], v154\n\t" \
  "v_exp_f32 v74, v74\n\t" \
  "v_exp_f32 v75, v75\n\t" \
  "v_exp_f32 v76, v76\n\t" \
  "v_exp_f32 v77, v77\n\t" \
  "v_exp_f32 v78, v78\n\t" \
  "v_exp_f32 v79, v79\n\t" \
  "v_exp_f32 v80, v80\n\t" \
  "v_exp_f32 v81, v81\n\t" \
  "v_smfmac_f32_32x32x32_bf16 v[96:111], v[148:151], v[72:79], v154\n\t" \
  "s_waitcnt lgkmcnt(0)\n\t" \
  "v_exp_f32 v82, v82\n\t" \
  "v_exp_f32 v83, v83\n\t" \
  "v_exp_f32 v84, v84\n\t" \
  "v_exp_f32 v85, v85\n\t" \
  "v_mfma_f32_32x32x16_bf16 v[64:79], v[136:139], v[128:131], 0\n\t" \
  "v_exp_f32 v86, v86\n\t" \
  "v_exp_f32 v87, v87\n\t" \
  "v_exp_f32 v88, v88\n\t" \
  "v_exp_f32 v89, v89\n\t" \
  "v_smfmac_f32_32x32x32_bf16 v[112:127], v[140:143], v[80:87], v154\n\t" \
  "s_mov_b64 exec, %[msk]\n\t" \
  "ds_read_b128 v[140:143], v153 offset:1024\n\t" \
  "ds_read_b128 v[144:147], v153 offset:1280\n\t" \
  "s_mov_b64 exec, -1\n\t" \
  "v_exp_f32 v90, v90\n\t" \
  "v_exp_f32 v91, v91\n\t" \
  "v_exp_f32 v92, v92\n\t" \
  "v_exp_f32 v93, v93\n\t" \
  "v_exp_f32 v94, v94\n\t" \
  "v_exp_f32 v95, v95\n\t" \
  "v_mov_b32 v152, v153\n\t" \
  "s_cbranch_scc1 1b\n\t" \
  "s_waitcnt lgkmcnt(0)\n\t" \
  "s_nop 4\n\t"

__global__ void __launch_bounds__(512, 2) k_fused(Params p) {
  const int g = blockIdx.x, tid = threadIdx.x, l = tid & 63, w = __builtin_amdgcn_readfirstlane(tid >> 6);
  const int rb = (g & 7) + 8 * (g >> 6), hg = (g >> 3) & 7;
  const int tc = ((g & 7) >> 1) * 4 + ((g >> 3) & 3), cg = (g & 1) * 8 + (g >> 5);
  const int h = l >> 5, G = l >> 4, r32 = l & 31;
  const __amdgpu_buffer_rsrc_t wsr = __builtin_amdgcn_make_buffer_rsrc(p.ws, 0, WS_SPAN, 0x00020000);
  const __amdgpu_buffer_rsrc_t outr = __builtin_amdgcn_make_buffer_rsrc(p.out + (size_t)rb * 64 * 1024 + hg * 64, 0, 64 * 4096, 0x00020000);
  const int kb = hg & 3, isv = hg >> 2;
  const u16* xbt = p.xb + (size_t)rb * 64 * 512;
#pragma unroll
  for (int i = 0; i < 2; i++) { const int r = kb * 16 + 2 * w + i; dma(xbt + r * 512, (l ^ (r & 15)) * 16, L_XB + r * 1024); }
#pragma unroll
  for (int i = 0; i < 2; i++) { const int pc = 2 * w + i; dma((isv ? p.wv : p.wk) + pc * 512, l * 16, L_WKV + pc * 1024); }
  if (w == 7 && l < 16) *(LAS unsigned*)(uintptr_t)(L_SM + l * 4) = 0;
#pragma unroll
  for (int i = 0; i < 6; i++) { int r = w * 6 + i; r += (r >= kb * 16) ? 16 : 0; dma(xbt + r * 512, (l ^ (r & 15)) * 16, L_XB + r * 1024); }
  {
    const unsigned vo = (l >> 3) * 1024 + (((l & 7) ^ (((l >> 4) & 1) << 2)) * 16);
#pragma unroll
    for (int i = 0; i < 8; i++) { const int pc = w * 8 + i; dma(p.wq + pc * 8 * 512 + hg * 64, vo, L_WQ + pc * 1024); }
  }
  u64 tag;
  asm volatile("s_load_dwordx2 %0, %1, 0x0\n\ts_waitcnt lgkmcnt(0)" : "=s"(tag) : "s"(p.ws + WS_EPOCH) : "memory");
  tag += 1;
  f32x16 o0, o1;
  if (w == 4) {
    const int key = l & 15, r = kb * 16 + key;
    lp xa[4];
#pragma unroll
    for (int i = 0; i < 4; i++) xa[i] = LP(L_XB + r * 1024 + (((4 * i + G) ^ key) * 16));
    const lp wa = LP(L_WKV + (8 * G + (key >> 2)) * 32 + (l & 3) * 8);
    WAIT_VM(14); bar();
    bf16x8 xf[16], wf[16];
#pragma unroll
    for (int s = 0; s < 16; s++) { xf[s] = AT(bf16x8, xa[s & 3], (s >> 2) * 256); wf[s] = tr2(wa, s * 1024, s * 1024 + 128); }
    f32x4 a0 = {0, 0, 0, 0}, a1 = a0;
    if (isv) {
#pragma unroll
      for (int s = 0; s < 16; s += 2) { a0 = mfma16(xf[s], wf[s], a0); a1 = mfma16(xf[s + 1], wf[s + 1], a1); }
    } else {
#pragma unroll
      for (int s = 0; s < 16; s += 2) { a0 = mfma16(wf[s], xf[s], a0); a1 = mfma16(wf[s + 1], xf[s + 1], a1); }
    }
    a0 += a1;
    const int kt = rb * 2 + (kb >> 1), hf = kb & 1;
    const unsigned off = isv ? 1024 + (hf + 2 * (G >> 1)) * 256 + key * 16 + (G & 1) * 8 : (hf * 16 + key + 32 * (G & 1)) * 16 + (G >> 1) * 8;
    st_dev64(p.ws + WS_KV + kt * 2048 + off, (u64)pk2(a0[0], a0[1]) | ((u64)pk2(a0[2], a0[3]) << 32));
    WAIT_VM(1); bar();
  } else {
    WAIT_VM(14); bar();
    WAIT_VM(0); bar();
  }
  const unsigned lane16 = l * 16;
  if (w < 4) {
    const int pt = w & 1, hp = w >> 1, q = (l & 15) >> 2, pp = l & 3;
    const int pos = pt * 32 + r32;
    lp xa[8];
#pragma unroll
    for (int i = 0; i < 8; i++) xa[i] = LP(L_XB + pos * 1024 + (((2 * i + h) ^ (pos & 15)) * 16));
    const int col = hp * 32 + (G & 1) * 16 + 4 * pp;
    const lp wa = LP(L_WQ + (8 * h + q) * 128 + (((col >> 3) ^ (((q >> 1) & 1) << 2)) * 16) + (col & 4) * 2);
    f32x16 acc = {0};
#pragma unroll
    for (int s = 0; s < 32; s++) acc = mfma32(tr2(wa, s * 2048, s * 2048 + 512), AT(bf16x8, xa[s & 7], (s >> 3) * 256), acc);
#pragma unroll
    for (int i = 0; i < 24; i++) __builtin_amdgcn_sched_group_barrier(0x100, 1, 0);
#pragma unroll
    for (int i = 0; i < 24; i++) { __builtin_amdgcn_sched_group_barrier(0x8, 1, 0); __builtin_amdgcn_sched_group_barrier(0x100, 3, 0); }
    asm volatile("" : "+v"(acc));
    y_add<Y_FREEQ>();
    constexpr float QS = 0.25f * 1.4426950408889634f;
    u32x4 q0 = {pk2(acc[0] * QS, acc[1] * QS), pk2(acc[2] * QS, acc[3] * QS), pk2(acc[4] * QS, acc[5] * QS), pk2(acc[6] * QS, acc[7] * QS)};
    u32x4 q1 = {pk2(acc[8] * QS, acc[9] * QS), pk2(acc[10] * QS, acc[11] * QS), pk2(acc[12] * QS, acc[13] * QS), pk2(acc[14] * QS, acc[15] * QS)};
    const unsigned one = (r32 == 16 || r32 == 20) ? 0x3F803F80u : 0u;
    const u32x4 vconst = {one, one, one, one};
    u32x4 vlo = vconst, vha = vconst, vhb = vconst;
    f32x16 X, Y = {0};
    o0 = Y; o1 = Y;
    const f32x16 zero = {0};
    unsigned idx = 0xDDDD, a = L_SA + lane16, b, cur = L_SA;
    y_wait<Y_RDYA>(4);
    const lp ap = LP(a);
    u32x4 kf = AT(u32x4, ap, 0);
    if ((l & 16) == 0) { vlo = AT(u32x4, ap, 1024); vha = AT(u32x4, ap, 1280); }
    X = mfma32(__builtin_bit_cast(bf16x8, kf), __builtin_bit_cast(bf16x8, q0), zero);
    const u64 msk = 0x0000FFFF0000FFFFull;
#pragma nounroll
    for (int sg = 0; sg < 5; sg++) {
      unsigned n = (0x91312u >> (4 * sg)) & 15, jmp = sg == 1 ? L_SB : cur + n * 8192;
      if (sg == 1) y_wait<Y_RDYB>(1);
      if (sg == 3) y_wait<Y_RDYC>(4);
      if (sg == 4) jmp = L_SC;
      asm volatile("s_nop 7\n\ts_nop 3\n\t" ATT_LOOP
        : "+{v[64:79]}"(X), "+{v[80:95]}"(Y), "+{v[96:111]}"(o0), "+{v[112:127]}"(o1), "+{v[136:139]}"(kf), "+{v[140:143]}"(vlo), "+{v[144:147]}"(vha), "+{v[148:151]}"(vhb),
          "+{v152}"(a), "=&{v153}"(b), [cur] "+s"(cur), [n] "+s"(n)
        : "{v[128:131]}"(q0), "{v[132:135]}"(q1), "{v154}"(idx), "{v155}"(lane16), [jmp] "s"(jmp), [msk] "s"(msk) : "scc", "memory");
      if (sg == 1) y_add<Y_FREEA>();
      if (sg == 3) y_add<Y_FREEB>();
    }
    o1 = smf(vhb, __builtin_shufflevector(Y, Y, 8, 9, 10, 11, 12, 13, 14, 15), o1, idx);
    if (__any(!(fminf(o0[8], o1[8]) >= 0x1p-64f && fmaxf(o0[8], o1[8]) <= 0x1p64f && o0[8] == o0[8] && o1[8] == o1[8]))) {
      float m0 = -INFINITY, m1 = -INFINITY;
      const bf16x8 qa = __builtin_bit_cast(bf16x8, q0), qb = __builtin_bit_cast(bf16x8, q1);
#pragma nounroll
      for (int kt = 0; kt < 64; kt++) {
        const bf16x8 k = __builtin_bit_cast(bf16x8, __builtin_amdgcn_raw_buffer_load_b128(wsr, lane16, WS_KV + kt * 2048, 16));
        const f32x16 s0 = mfma32(k, qa, zero), s1 = mfma32(k, qb, zero);
#pragma unroll
        for (int j = 0; j < 16; j++) { m0 = fmaxf(m0, s0[j]); m1 = fmaxf(m1, s1[j]); }
      }
      m0 = fmaxf(m0, __shfl_xor(m0, 32)); m1 = fmaxf(m1, __shfl_xor(m1, 32));
      o0 = zero; o1 = zero;
#pragma nounroll
      for (int kt = 0; kt < 64; kt++) {
        const bf16x8 k = __builtin_bit_cast(bf16x8, __builtin_amdgcn_raw_buffer_load_b128(wsr, lane16, WS_KV + kt * 2048, 16));
        u32x4 v0 = __builtin_amdgcn_raw_buffer_load_b128(wsr, lane16 & ~256u, WS_KV + kt * 2048 + 1024, 16), v1 = __builtin_amdgcn_raw_buffer_load_b128(wsr, lane16 & ~256u, WS_KV + kt * 2048 + 1280, 16);
        if (l & 16) { v0 = vconst; v1 = vconst; }
        const f32x16 s0 = mfma32(k, qa, zero), s1 = mfma32(k, qb, zero);
        f32x8 pa, pb, pc, pd;
#pragma unroll
        for (int j = 0; j < 8; j++) { pa[j] = ex2(s0[j] - m0); pb[j] = ex2(s0[8 + j] - m0); pc[j] = ex2(s1[j] - m1); pd[j] = ex2(s1[8 + j] - m1); }
        o0 = smf(v0, pa, o0, idx); o0 = smf(v1, pb, o0, idx); o1 = smf(v0, pc, o1, idx); o1 = smf(v1, pd, o1, idx);
      }
    }
    const float i0 = 1.f / o0[8], i1 = 1.f / o1[8];
    u32x4 f0, f1;
#pragma unroll
    for (int j = 0; j < 4; j++) { f0[j] = min(pk2(o0[2 * j] * i0, o0[2 * j + 1] * i0), 0xFFFFFFFEu); f1[j] = min(pk2(o1[2 * j] * i1, o1[2 * j + 1] * i1), 0xFFFFFFFEu); }
    const unsigned oo = WS_OF + ((rb * 2 + pt) * 32 + hg * 4 + hp * 2) * 1024;
    __builtin_amdgcn_raw_buffer_store_b128(f0, wsr, lane16, oo, 16);
    __builtin_amdgcn_raw_buffer_store_b128(f1, wsr, lane16, oo + 1024, 16);
  } else {
    const int a = w - 4;
    __builtin_amdgcn_s_setprio(1);
    if (a == 0) {
      WAIT_VM(0);
      if (l < 32) st_dev64(p.ws + WS_KVF + l * 2048 + (rb * 8 + hg) * 8, tag);
      if (l < 32) {
        const int ch = cg * 32 + l;
        const float ar = bf2f(p.lre[ch]), ai = bf2f(p.lim[ch]), dt = (float)(__bf16)expf(bf2f(p.ldt[ch]));
        float e = expf(dt * ar), sn, cs; sincosf(dt * ai, &sn, &cs);
        const float lr = e * cs, li = e * sn, nr = lr - 1.f, inv = 2.f / (ar * ar + ai * ai);
        float pr = lr, pi = li; csq(pr, pi); csq(pr, pi); csq(pr, pi);
        float qr = pr, qi = pi; csq(qr, qi); csq(qr, qi); csq(qr, qi); csq(qr, qi);
        const lp lt = LP(L_LAM + l * 32);
        AT(f32x4, lt, 0) = (f32x4){lr, li, (nr * ar + li * ai) * inv, (li * ar - nr * ai) * inv};
        AT(f32x4, lt, 16) = (f32x4){pr, pi, qr, qi};
      }
    } else if (a == 1) {
      const char* fp = p.ws + WS_KVF + (g & 31) * 2048 + l * 8;
      _Pragma("nounroll") for (int it = 0; it < SPIN; it++) {
        const u64 f0 = ld_dev64(fp), f1 = ld_dev64(fp + 512), f2 = ld_dev64(fp + 1024), f3 = ld_dev64(fp + 1536);
        if (__all(f0 == tag && f1 == tag && f2 == tag && f3 == tag)) break;
        __builtin_amdgcn_s_sleep(1);
      }
      y_add<Y_KVOK>();
      if (g == 0 && l == 0) st_dev64(p.ws + WS_EPOCH, tag);
    } else {
      const u32x4 ps = {~0u, ~0u, ~0u, ~0u};
#pragma unroll
      for (int i = 0; i < 4; i++) __builtin_amdgcn_raw_buffer_store_b128(ps, wsr, lane16, WS_OF + ((rb * 2 + a - 2) * 32 + hg * 4 + i) * 1024, 16);
    }
    y_wait<Y_KVOK>(1);
    const char* kv = p.ws + WS_KV;
#pragma unroll
    for (int i = 0; i < 6; i++) dma_dev(kv + (i * 4 + a) * 1024, lane16, L_SA + (i * 4 + a) * 1024);
    WAIT_VM(0);
    y_add<Y_RDYA>();
    y_wait<Y_FREEQ>(4);
#pragma unroll
    for (int i = 0; i < 8; i++) dma_dev(kv + 24576 + (a * 8 + i) * 1024, lane16, L_SB + (a * 8 + i) * 1024);
    const int i8 = l >> 3;
    const unsigned sw0 = ((l & 7) ^ G) * 16, sw1 = ((l & 7) ^ (4 + G)) * 16;
    const unsigned ax0 = (16 * (i8 >> 2) + (i8 & 3)) * 1024 + sw0, ax1 = ax0 - sw0 + sw1, bx0 = i8 * 1024 + sw0, bx1 = i8 * 1024 + sw1;
    const u16* xat = p.xa + (size_t)(tc * 128 + a * 32) * 512;
    const u16* bt = ((a & 1) ? p.bim : p.bre) + (size_t)(cg * 32 + (a >> 1) * 16) * 512;
    auto g1_issue = [&](int c) {
      const unsigned sl = L_RING + (c & 3) * 24576;
#pragma unroll
      for (int i = 0; i < 4; i++) dma(xat + i * 4 * 512 + c * 64, (i & 1) ? ax1 : ax0, sl + (a * 4 + i) * 1024);
#pragma unroll
      for (int i = 0; i < 2; i++) dma(bt + i * 8 * 512 + c * 64, i ? bx1 : bx0, sl + 16384 + (a * 2 + i) * 1024);
    };
#pragma unroll
    for (int c = 0; c < 4; c++) g1_issue(c);
    const unsigned fr = (r32 >> 1) & 7;
    const unsigned rdo = r32 * 128;
    unsigned so[4];
#pragma unroll
    for (int s = 0; s < 4; s++) so[s] = rdo + (((2 * s + h) ^ fr) * 16);
    lp ga[4], gb[4];
#pragma unroll
    for (int s = 0; s < 4; s++) { ga[s] = LP(L_RING + a * 4096 + so[s]); gb[s] = LP(L_RING + 16384 + so[s]); }
    f32x16 u0 = {0}, u1 = {0};
#pragma unroll
    for (int c = 0; c < 8; c++) {
      if (c == 0) WAIT_VM(18); else WAIT_VM(12);
      y_add<Y_GRP>(); y_wait<Y_GRP>(4 * (c + 1));
      if (c == 0 && a == 0) y_add<Y_RDYB>();
      if (c >= 1 && c <= 4) g1_issue(c + 3);
      if (c >= 5) {
#pragma unroll
        for (int i = 0; i < 6; i++) dma_dev(kv + 57344 + ((c - 5) * 24 + a * 6 + i) * 1024, lane16, L_SC + ((c - 5) * 24 + a * 6 + i) * 1024);
      }
      bf16x8 af[4], b0[4], b1[4];
      if ((c & 3) == 2 || c == 4) {
#pragma unroll
        for (int s = 0; s < 4; s++) { ga[s] += c == 4 ? -49152 : 49152; gb[s] += c == 4 ? -49152 : 49152; asm("" : "+v"(ga[s]), "+v"(gb[s])); }
      }
#pragma unroll
      for (int s = 0; s < 4; s++) { af[s] = AT(bf16x8, ga[s], (c & 1) * 24576); b0[s] = AT(bf16x8, gb[s], (c & 1) * 24576); b1[s] = AT(bf16x8, gb[s], (c & 1) * 24576 + 4096); }
#pragma unroll
      for (int s = 0; s < 4; s++) { u0 = mfma32(af[s], b0[s], u0); u1 = mfma32(af[s], b1[s], u1); }
      asm volatile("" : "+v"(u0), "+v"(u1));
    }
    float ur[2][8], ui[2][8], lr[2], li[2], zr[2], zi[2];
#pragma unroll
    for (int j = 0; j < 8; j++) {
      auto s0 = __builtin_amdgcn_permlane16_swap(f2u(u0[j]), f2u(u0[j + 8]), false, false); ur[0][j] = u2f(s0[0]); ui[0][j] = u2f(s0[1]);
      auto s1 = __builtin_amdgcn_permlane16_swap(f2u(u1[j]), f2u(u1[j + 8]), false, false); ur[1][j] = u2f(s1[0]); ui[1][j] = u2f(s1[1]);
    }
    const int seg = 4 * a + G;
    const lp pl = LP(L_LAM + (l & 15) * 32), pse = LP(L_SE + (seg * 32 + (l & 15)) * 8);
#pragma unroll
    for (int n = 0; n < 2; n++) {
      const f32x4 lm = AT(f32x4, pl, n * 512);
      lr[n] = lm[0]; li[n] = lm[1]; zr[n] = lm[2]; zi[n] = lm[3];
      float er = ur[n][0], ei = ui[n][0];
#pragma unroll
      for (int j = 1; j < 8; j++) cmac(lr[n], li[n], er, ei, ur[n][j], ui[n][j]);
      AT(f32x2, pse, n * 128) = (f32x2){er, ei};
    }
    y_add<Y_GRP>();
    if (a == 0) {
      y_wait<Y_GRP>(36);
      const f32x4 pw = AT(f32x4, LP(L_LAM + r32 * 32), 16);
      const lp ps = LP(L_SE + r32 * 8), pc = LP(L_CS + r32 * 8);
      f32x2 se[16];
#pragma unroll
      for (int s = 0; s < 16; s++) se[s] = AT(f32x2, ps, s * 256);
      float cr = se[0][0], ci = se[0][1];
#pragma unroll
      for (int s = 1; s < 16; s++) cmac(pw[0], pw[1], cr, ci, se[s][0], se[s][1]);
      if (l < 32) st_dev64(p.ws + WS_E + (tc * 512 + cg * 32 + l) * 8, (u64)f2u(cr) | ((u64)f2u(ci) << 32));
      WAIT_VM(0);
      if (l == 0) st_dev64(p.ws + WS_EF + (cg * 16 + tc) * 8, tag);
      y_add<Y_RDYC>();
      const char* fp = p.ws + WS_EF + (cg * 16 + (l & 15)) * 8;
      _Pragma("nounroll") for (int it = 0; it < SPIN; it++) {
        if (__all((l & 15) >= tc || ld_dev64(fp) == tag)) break;
        __builtin_amdgcn_s_sleep(2);
      }
      u64 ee[15];
#pragma unroll
      for (int t = 0; t < 15; t++) ee[t] = ld_dev64(p.ws + WS_E + (min(t, tc) * 512 + cg * 32 + r32) * 8);
      cr = 0; ci = 0;
#pragma unroll
      for (int t = 0; t < 15; t++) if (t < tc) cmac(pw[2], pw[3], cr, ci, u2f((unsigned)ee[t]), u2f((unsigned)(ee[t] >> 32)));
#pragma unroll
      for (int s = 0; s < 16; s++) { AT(f32x2, pc, s * 256) = (f32x2){cr, ci}; cmac(pw[0], pw[1], cr, ci, se[s][0], se[s][1]); }
      y_add<Y_CAR>();
    } else {
      WAIT_VM(0);
      y_add<Y_RDYC>();
      y_wait<Y_CAR>(1);
    }
    const unsigned zt = L_LEFT + a * 4096;
    const lp pcs = LP(L_CS + (seg * 32 + (l & 15)) * 8), pz = LP(zt + (l & 15) * 64 + G * 16);
#pragma unroll
    for (int n = 0; n < 2; n++) {
      const f32x2 c = AT(f32x2, pcs, n * 128);
      float hr = c[0], hi = c[1], yr[8], yi[8];
#pragma unroll
      for (int j = 0; j < 8; j++) { cmac(lr[n], li[n], hr, hi, ur[n][j], ui[n][j]); yr[j] = zr[n] * hr - zi[n] * hi; yi[j] = -(zr[n] * hi + zi[n] * hr); }
      AT(u32x4, pz, n * 1024) = (u32x4){pk2(yr[0], yr[1]), pk2(yr[2], yr[3]), pk2(yr[4], yr[5]), pk2(yr[6], yr[7])};
      AT(u32x4, pz, 2048 + n * 1024) = (u32x4){pk2(yi[0], yi[1]), pk2(yi[2], yi[3]), pk2(yi[4], yi[5]), pk2(yi[6], yi[7])};
    }
    {
      const lp za = LP(zt + (8 * h + ((l & 15) >> 2)) * 64 + (G & 1) * 32 + (l & 3) * 8);
      const unsigned ho = WS_HT + ((tc * 4 + a) * 64 + cg * 2) * 1024;
#pragma unroll
      for (int i = 0; i < 4; i++) __builtin_amdgcn_raw_buffer_store_b128(__builtin_bit_cast(u32x4, tr2(za, i * 1024, i * 1024 + 256)), wsr, lane16, ho + ((i >> 1) * 32 + (i & 1)) * 1024, 16);
    }
    WAIT_VM(0);
    y_add<Y_GRP>(); y_wait<Y_GRP>(40);
    if (a == 0 && l == 0) st_dev64(p.ws + WS_HF + (tc * 16 + cg) * 8, tag);
    const unsigned s0 = a < 3 ? L_LEFT + a * 8192 : L_SA, s1 = L_SB + a * 8192;
    const u16* ct = ((a >> 1) ? p.cim : p.cre) + (size_t)hg * 64 * 512 + (a & 1) * 256;
    auto c_issue = [&](int c, unsigned sl) {
#pragma unroll
      for (int i = 0; i < 8; i++) dma(ct + i * 8 * 512 + c * 64, (i & 1) ? bx1 : bx0, sl + i * 1024);
    };
    y_wait<Y_FREEA>(4);
    c_issue(0, s0);
    const int omt = a & 1, ont = a >> 1, od = hg * 64 + ont * 32 + r32;
    const __amdgpu_buffer_rsrc_t xar = __builtin_amdgcn_make_buffer_rsrc((void*)(p.xa + (size_t)(rb * 64 + omt * 32) * 512 + hg * 64 + ont * 32), 0, 32 * 1024, 0x00020000);
    u16 xs[16];
#pragma unroll
    for (int j = 0; j < 16; j++) xs[j] = __builtin_amdgcn_raw_buffer_load_b16(xar, h * 4096 + r32 * 2, ((j & 3) + 8 * (j >> 2)) * 1024, 0);
    const float dk = bf2f(p.dsk[od]);
    y_wait<Y_FREEB>(4);
    c_issue(1, s1);
    if (a == 1) {
      const int g2 = (g & ~56) | ((l & 7) << 3), tc2 = ((g2 & 7) >> 1) * 4 + ((g2 >> 3) & 3), cg2 = (g2 & 1) * 8 + (g2 >> 5);
      const char* fp = p.ws + ((l & 16) ? WS_EF + (cg2 * 16 + tc2) * 8 : WS_HF + ((rb >> 1) * 16 + (l & 15)) * 8);
      _Pragma("nounroll") for (int it = 0; it < SPIN; it++) {
        if (__all(ld_dev64(fp) == tag)) break;
        __builtin_amdgcn_s_sleep(2);
      }
      y_add<Y_HOK>();
    } else y_wait<Y_HOK>(1);
    u32x4 hf[16][2];
#pragma unroll
    for (int s = 0; s < 16; s++)
#pragma unroll
      for (int m = 0; m < 2; m++) hf[s][m] = __builtin_amdgcn_raw_buffer_load_b128(wsr, lane16, WS_HT + ((rb * 2 + m) * 64 + a * 16 + s) * 1024, 16);
    f32x16 y[2][2] = {{{0}, {0}}, {{0}, {0}}};
#pragma unroll
    for (int c = 0; c < 4; c++) {
      if (c == 0) WAIT_VM(32); else if (c == 2) WAIT_VM(8); else if (c == 3) WAIT_VM(0);
      const unsigned sl = (c & 1) ? s1 : s0;
      bf16x8 c0[4], c1[4];
#pragma unroll
      for (int s = 0; s < 4; s++) { const lp e = LP(sl + so[s]); c0[s] = AT(bf16x8, e, 0); c1[s] = AT(bf16x8, e, 4096); }
#pragma unroll
      for (int s = 0; s < 4; s++)
#pragma unroll
        for (int m = 0; m < 2; m++) { const bf16x8 hv = __builtin_bit_cast(bf16x8, hf[c * 4 + s][m]); y[m][0] = mfma32(hv, c0[s], y[m][0]); y[m][1] = mfma32(hv, c1[s], y[m][1]); }
      asm volatile("" : "+v"(y[0][0]), "+v"(y[0][1]), "+v"(y[1][0]), "+v"(y[1][1]));
      if (c < 2) c_issue(c + 2, sl);
    }
    {
#pragma unroll
      for (int d = 0; d < 4; d++) if (d != a) {
        const int pc = (d - a - 1) & 3;
        const lp e = LP((pc < 2 ? s0 + pc * 4096 : s1) + lane16);
        const f32x16 v = y[d & 1][d >> 1];
#pragma unroll
        for (int i = 0; i < 4; i++) AT(f32x4, e, i * 1024) = (f32x4){v[4 * i], v[4 * i + 1], v[4 * i + 2], v[4 * i + 3]};
      }
      f32x16 t = a == 0 ? y[0][0] : a == 1 ? y[1][0] : a == 2 ? y[0][1] : y[1][1];
      y_add<Y_GRP>(); y_wait<Y_GRP>(44);
#pragma unroll
      for (int k = 1; k < 4; k++) {
        const int sa = (a + k) & 3, pc = 3 - k;
        const lp e = LP((pc < 2 ? (sa < 3 ? L_LEFT + sa * 8192 : L_SA) + pc * 4096 : L_SB + sa * 8192) + lane16);
#pragma unroll
        for (int i = 0; i < 4; i++) { const f32x4 v = AT(f32x4, e, i * 1024); t[4 * i] += v[0]; t[4 * i + 1] += v[1]; t[4 * i + 2] += v[2]; t[4 * i + 3] += v[3]; }
      }
      asm volatile("" : "+v"(t));
      y_add<Y_GRP>();
#pragma unroll
      for (int j = 0; j < 16; j++)
        __builtin_amdgcn_raw_buffer_store_b32(f2u(t[j] + (float)(__bf16)(bf2f(xs[j]) * dk)), outr, h * 16384 + r32 * 4, (omt * 32 + (j & 3) + 8 * (j >> 2)) * 4096 + ont * 128, OUTA_AUX);
      y_wait<Y_GRP>(48);
    }
    {
      const unsigned vo = (l >> 3) * 1024 + (((l & 7) ^ (((l >> 4) & 1) << 2)) * 16);
#pragma unroll
      for (int i = 0; i < 16; i++) dma(p.wo + (a * 128 + i * 8) * 512 + hg * 64, vo, (i < 8 ? s0 : s1) + (i & 7) * 1024);
      WAIT_VM(0);
    }
    __builtin_amdgcn_s_setprio(0);
  }
  {
    const int kq = w & 3, mt = w >> 2, q = (l & 15) >> 2, pp = l & 3;
    const unsigned ob = WS_OF + ((rb * 2 + mt) * 32 + kq * 8) * 1024;
    u32x4 of[8];
    y_wait<Y_HOK>(1);
#pragma unroll
    for (int s = 0; s < 8; s++) of[s] = __builtin_amdgcn_raw_buffer_load_b128(wsr, lane16, ob + s * 1024, 16);
    bar();
    const unsigned s0 = kq < 3 ? L_LEFT + kq * 8192 : L_SA, s1 = L_SB + kq * 8192;
    bf16x8 wf[8][2];
#pragma unroll
    for (int n = 0; n < 2; n++) {
      const int col = n * 32 + (G & 1) * 16 + 4 * pp;
      const unsigned wo = (4 * h + q) * 128 + (((col >> 3) ^ (((q >> 1) & 1) << 2)) * 16) + (col & 4) * 2;
      const lp e0 = LP(s0 + wo), e1 = LP(s1 + wo);
#pragma unroll
      for (int s = 0; s < 8; s++) wf[s][n] = tr2(s < 4 ? e0 : e1, (s & 3) * 2048, (s & 3) * 2048 + 1024);
    }
    _Pragma("nounroll") for (int it = 0; it < SPIN; it++) {
      unsigned mx = 0;
#pragma unroll
      for (int s = 0; s < 8; s++) mx = max(max(mx, max(of[s][0], of[s][1])), max(of[s][2], of[s][3]));
      if (!__any(mx == ~0u)) break;
      __builtin_amdgcn_s_sleep(1);
#pragma unroll
      for (int s = 0; s < 8; s++) of[s] = __builtin_amdgcn_raw_buffer_load_b128(wsr, lane16, ob + s * 1024, 16);
    }
    f32x16 y0 = {0}, y1 = {0};
#pragma unroll
    for (int s = 0; s < 8; s++) { const bf16x8 ov = __builtin_bit_cast(bf16x8, of[s]); y0 = mfma32(ov, wf[s][0], y0); y1 = mfma32(ov, wf[s][1], y1); }
    const lp ew = LP(L_EX + (mt * 16 + kq) * 2048 + lane16);
#pragma unroll
    for (int d = 0; d < 4; d++) if (d != kq) {
      const f32x16 v = (d >> 1) ? y1 : y0;
      const int o = (d & 1) * 8;
      AT(f32x4, ew, d * 8192) = (f32x4){v[o], v[o + 1], v[o + 2], v[o + 3]};
      AT(f32x4, ew, d * 8192 + 1024) = (f32x4){v[o + 4], v[o + 5], v[o + 6], v[o + 7]};
    }
    const f32x16 mv = (kq >> 1) ? y1 : y0;
    float t[8];
#pragma unroll
    for (int j = 0; j < 8; j++) t[j] = (kq & 1) ? mv[8 + j] : mv[j];
    bar();
#pragma unroll
    for (int k = 1; k < 4; k++) {
      const lp e = LP(L_EX + ((mt * 4 + kq) * 4 + ((kq + k) & 3)) * 2048 + lane16);
      const f32x4 v0 = AT(f32x4, e, 0), v1 = AT(f32x4, e, 1024);
#pragma unroll
      for (int j = 0; j < 4; j++) { t[j] += v0[j]; t[4 + j] += v1[j]; }
    }
#pragma unroll
    for (int j = 0; j < 8; j++)
      __builtin_amdgcn_raw_buffer_store_b32(f2u(t[j]), outr, h * 16384 + r32 * 4, (mt * 32 + (kq & 1) * 16 + (j & 3) + 8 * (j >> 2)) * 4096 + 2048 + (kq >> 1) * 128, OUTB_AUX);
  }
}

extern "C" void kernel_launch(void* const* d_in, const int* in_sizes, int n_in, void* d_out, int out_size, void* d_ws, size_t ws_size, hipStream_t stream) {
  Params p;
  p.xa = (const u16*)d_in[0]; p.lre = (const u16*)d_in[1]; p.lim = (const u16*)d_in[2]; p.bre = (const u16*)d_in[3]; p.bim = (const u16*)d_in[4];
  p.cre = (const u16*)d_in[5]; p.cim = (const u16*)d_in[6]; p.ldt = (const u16*)d_in[7]; p.dsk = (const u16*)d_in[8]; p.xb = (const u16*)d_in[9];
  p.wq = (const u16*)d_in[10]; p.wk = (const u16*)d_in[11]; p.wv = (const u16*)d_in[12]; p.wo = (const u16*)d_in[13];
  p.out = (float*)d_out; p.ws = (char*)d_ws;
  (void)hipFuncSetAttribute((const void*)k_fused, hipFuncAttributeMaxDynamicSharedMemorySize, 163840);
  k_fused<<<256, 512, 163840, stream>>>(p);
}
```

```cpp
#define OUTB_AUX 2
#define OUTA_AUX 2
#include <hip/hip_runtime.h>
#include <hip/hip_bf16.h>
#include <stdint.h>

typedef float f32x2 __attribute__((ext_vector_type(2)));
typedef float f32x4 __attribute__((ext_vector_type(4)));
typedef float f32x8 __attribute__((ext_vector_type(8)));
typedef float f32x16 __attribute__((ext_vector_type(16)));
typedef __bf16 bf16x2 __attribute__((ext_vector_type(2)));
typedef __bf16 bf16x8 __attribute__((ext_vector_type(8)));
typedef __bf16 bf16x16 __attribute__((ext_vector_type(16)));
typedef short s16x4 __attribute__((ext_vector_type(4)));
typedef unsigned u32x2 __attribute__((ext_vector_type(2)));
typedef unsigned u32x4 __attribute__((ext_vector_type(4)));
typedef unsigned short u16;
typedef unsigned long long u64;
#define LAS __attribute__((address_space(3)))
#define GAS __attribute__((address_space(1)))
#define KVREP 32
#define ROT 13
#define HAHEAD 6
#define PDLY 12
#define PF 0
#define PFL 32
#define PFC 2
#define PFW 6
#define PFO 0
#define HFS 128
#define GP 0
#define SDE 1
#define PFX 0
#define BW 4
#define CT 1
#define CPF 2
#define CPW 7
#define SBF 2
#define PSK 0
#define SEL 1
#define QS_ 0
#define LS_ 2
#define ESL 12
#define WF 1
#define SLP "1"
#define DEV __device__ __forceinline__
#define RLX_AGENT __ATOMIC_RELAXED, __HIP_MEMORY_SCOPE_AGENT

constexpr unsigned WS_EPOCH = 0x0;
constexpr unsigned WS_HF = 0x60000;
constexpr unsigned WS_KVF = 0x10000;
constexpr unsigned WS_E = 0x20000;
constexpr unsigned WS_KV = 0x40000;
constexpr unsigned WS_OF = 0x100000;
constexpr unsigned WS_HT = 0x400000;
constexpr unsigned WS_SPAN = 0x800000;

constexpr unsigned L_XB = 0, L_WQ = 65536, L_WKV = 131072;
constexpr unsigned L_SA = 131072, L_SB = 0, L_SC = 32768, L_SD = 65536, L_LEFT = 106496;
constexpr unsigned L_GB = 32768, L_GX = 81920;
constexpr unsigned L_SE = 155648;
constexpr unsigned L_SM = 159744;
constexpr unsigned L_LAM = L_SM + 256;
constexpr unsigned L_DUMMY = L_SM + 2560;
constexpr unsigned Y_KVOK = L_SM, Y_RDYA = L_SM + 4, Y_RDYB = L_SM + 8, Y_RDYC = L_SM + 12, Y_RDYD = L_SM + 16, Y_FREEA = L_SM + 20, Y_FREEB = L_SM + 24, Y_FREEC = L_SM + 28,
                   Y_FREEQ = L_SM + 32, Y_GRP = L_SM + 36, Y_CAR = L_SM + 40, Y_HOK = L_SM + 44, Y_RDYQ = L_SM + 48, Y_OOK = L_SM + 52, Y_GRQ = L_SM + 56, Y_BEND = L_SM + 60;

struct Params {
  const u16 *xa, *lre, *lim, *bre, *bim, *cre, *cim, *ldt, *dsk, *xb, *wq, *wk, *wv, *wo;
  float* out; char* ws;
};

DEV unsigned f2u(float f) { return __builtin_bit_cast(unsigned, f); }
DEV float u2f(unsigned u) { return __builtin_bit_cast(float, u); }
DEV float bf2f(u16 h) { return __builtin_bit_cast(float, (unsigned)h << 16); }
DEV unsigned pk2(float a, float b) { f32x2 f = {a, b}; return __builtin_bit_cast(unsigned, __builtin_convertvector(f, bf16x2)); }
DEV void dma(const void* base, unsigned voff, unsigned lds) {
  asm volatile("s_nop 0\n\tglobal_load_lds_dwordx4 %0, %1" :: "v"(voff), "s"(base), "{m0}"(lds) : "memory");
}
DEV void dma_dev(const void* base, unsigned voff, unsigned lds) {
  asm volatile("s_nop 0\n\tglobal_load_lds_dwordx4 %0, %1 sc1" :: "v"(voff), "s"(base), "{m0}"(lds) : "memory");
}
#define DMA4(SC) "s_nop 0\n\tglobal_load_lds_dwordx4 %0, %4" SC "\n\tglobal_load_lds_dwordx4 %1, %4 offset:1024" SC "\n\tglobal_load_lds_dwordx4 %2, %4 offset:2048" SC "\n\tglobal_load_lds_dwordx4 %3, %4 offset:3072" SC
DEV void dma4(const void* base, unsigned v0, unsigned v1, unsigned v2, unsigned v3, unsigned lds) {
  asm volatile(DMA4("") :: "v"(v0), "v"(v1), "v"(v2), "v"(v3), "s"(base), "{m0}"(lds) : "memory");
}
DEV void dma4_dev(const void* base, unsigned v, unsigned lds) { asm volatile(DMA4(" sc1") :: "v"(v), "v"(v), "v"(v), "v"(v), "s"(base), "{m0}"(lds) : "memory"); }
DEV void dma2(const void* base, unsigned v0, unsigned v1, unsigned lds) {
  asm volatile("s_nop 0\n\tglobal_load_lds_dwordx4 %0, %2\n\tglobal_load_lds_dwordx4 %1, %2 offset:1024" :: "v"(v0), "v"(v1), "s"(base), "{m0}"(lds) : "memory");
}
DEV void dma2_dev(const void* base, unsigned v, unsigned lds) {
  asm volatile("s_nop 0\n\tglobal_load_lds_dwordx4 %0, %1 sc1\n\tglobal_load_lds_dwordx4 %0, %1 offset:1024 sc1" :: "v"(v), "s"(base), "{m0}"(lds) : "memory");
}
DEV void touch(const void* base, unsigned voff) {
  asm volatile("s_nop 0\n\tglobal_load_lds_dword %0, %1" :: "v"(voff), "s"(base), "{m0}"(L_DUMMY) : "memory");
}
#define WAIT_VMX(n) asm volatile("s_waitcnt vmcnt(%0)" :: "n"(n) : "memory")
#define WAIT_VM(n) asm volatile("s_waitcnt vmcnt(" #n ")" ::: "memory")
DEV void bar() { asm volatile("s_waitcnt lgkmcnt(0)\n\ts_barrier" ::: "memory"); }
typedef LAS char* lp;
DEV lp LP(unsigned a) { lp q = (lp)(uintptr_t)a; asm("" : "+v"(q)); return q; }
#define AT(T, q, off) (*(LAS T*)((q) + (off)))
DEV bf16x8 tr2(lp q, int o0, int o1) {
  s16x4 x = __builtin_amdgcn_ds_read_tr16_b64_v4i16((LAS s16x4*)(q + o0));
  s16x4 y = __builtin_amdgcn_ds_read_tr16_b64_v4i16((LAS s16x4*)(q + o1));
  return __builtin_bit_cast(bf16x8, __builtin_shufflevector(x, y, 0, 1, 2, 3, 4, 5, 6, 7));
}
DEV f32x16 mfma32(bf16x8 a, bf16x8 b, f32x16 c) { return __builtin_amdgcn_mfma_f32_32x32x16_bf16(a, b, c, 0, 0, 0); }
DEV f32x4 mfma16(bf16x8 a, bf16x8 b, f32x4 c) { return __builtin_amdgcn_mfma_f32_16x16x32_bf16(a, b, c, 0, 0, 0); }
DEV f32x16 smf(u32x4 a, f32x8 b, f32x16 c, int idx) { return __builtin_amdgcn_smfmac_f32_32x32x32_bf16(__builtin_bit_cast(bf16x8, a), __builtin_bit_cast(bf16x16, b), c, idx, 0, 0); }
DEV float ex2(float x) { return __builtin_amdgcn_exp2f(x); }
#define y_add(A) asm volatile("s_waitcnt lgkmcnt(0)\n\ts_mov_b64 exec, 1\n\tds_add_u32 %0, %1 offset:%2\n\ts_mov_b64 exec, -1" :: "v"(vsm), "v"(vone), "n"((A) - L_SM) : "memory")
#define y_addn(A) asm volatile("s_mov_b64 exec, 1\n\tds_add_u32 %0, %1 offset:%2\n\ts_mov_b64 exec, -1" :: "v"(vsm), "v"(vone), "n"((A) - L_SM) : "memory")
template <unsigned A, int S> DEV void y_wait_(unsigned vsm, unsigned target) {
  unsigned v, sv, n = 1u << 22;
  asm volatile("1:\n\tds_read_b32 %0, %3 offset:%4\n\ts_waitcnt lgkmcnt(0)\n\ts_setprio 1\n\tv_readfirstlane_b32 %1, %0\n\ts_setprio 0\n\ts_cmp_ge_u32 %1, %5\n\ts_cbranch_scc1 2f\n\t"
               "s_sleep %6\n\ts_sub_u32 %2, %2, 1\n\ts_cbranch_scc0 1b\n2:"
               : "=&v"(v), "=&s"(sv), "+&s"(n) : "v"(vsm), "n"(A - L_SM), "s"(target), "n"(S) : "memory", "scc");
}
#define y_wait(A, t) y_wait_<A, 1>(vsm, t)
#define y_waitq(A, t) y_wait_<A, QS_>(vsm, t)
#define y_waitl(A, t) y_wait_<A, LS_>(vsm, t)
#define PRIO(n) { __builtin_amdgcn_sched_barrier(0); __builtin_amdgcn_s_setprio(n); __builtin_amdgcn_sched_barrier(0); }
#define MP 0
#define MPRIO(n) FENCE;
#define MPRIO1(n) FENCE;
#define FENCE __builtin_amdgcn_sched_barrier(0)
DEV u64 ld_dev64(const char* p) { return __hip_atomic_load((GAS u64*)p, RLX_AGENT); }
DEV void st_dev64(char* p, u64 v) { __hip_atomic_store((GAS u64*)p, v, RLX_AGENT); }
constexpr int SPIN = 1 << 16;
DEV void cmac(float ar, float ai, float& r, float& i, float ur, float ui) {
  float tr = fmaf(ar, r, ur), ti = fmaf(ar, i, ui); tr = fmaf(-ai, i, tr); ti = fmaf(ai, r, ti); r = tr; i = ti;
}
DEV f32x2 pfma(f32x2 a, f32x2 b, f32x2 c) { return __builtin_elementwise_fma(a, b, c); }
DEV f32x2 cpk(f32x2 a, f32x2 x, f32x2 u) {
  asm("v_pk_fma_f32 %0, %1, %2, %0 op_sel_hi:[0,1,1]\n\tv_pk_fma_f32 %0, %1, %2, %0 op_sel:[1,1,0] op_sel_hi:[1,0,1] neg_lo:[1,0,0]" : "+&v"(u) : "v"(a), "v"(x));
  return u;
}
DEV void csq(float& r, float& i) { float t = r * r - i * i; i = 2.f * r * i; r = t; }

#define ATT_LOOP \
  ".p2align 6\n\t" \
  "1:\n\t" \
  "v_exp_f32 v64, v64\n\t" \
  "v_exp_f32 v65, v65\n\t" \
  "v_smfmac_f32_32x32x32_bf16 v[112:127], v[172:175], v[88:95], v154\n\t" \
  "s_add_u32 %[cur], %[cur], 0x2000\n\t" \
  "s_sub_u32 %[n], %[n], 1\n\t" \
  "v_exp_f32 v66, v66\n\t" \
  "v_exp_f32 v67, v67\n\t" \
  "v_exp_f32 v68, v68\n\t" \
  "v_exp_f32 v69, v69\n\t" \
  "v_mfma_f32_32x32x16_bf16 v[80:95], v[136:139], v[132:135], 0\n\t" \
  "s_waitcnt lgkmcnt(3)\n\t" \
  "ds_read_b128 v[136:139], v152 offset:4096\n\t" \
  "v_exp_f32 v70, v70\n\t" \
  "v_exp_f32 v71, v71\n\t" \
  "v_exp_f32 v72, v72\n\t" \
  "v_exp_f32 v73, v73\n\t" \
  "v_smfmac_f32_32x32x32_bf16 v[96:111], v[140:143], v[64:71], v154\n\t" \
  "v_exp_f32 v74, v74\n\t" \
  "v_exp_f32 v75, v75\n\t" \
  "v_exp_f32 v76, v76\n\t" \
  "v_exp_f32 v77, v77\n\t" \
  "v_exp_f32 v78, v78\n\t" \
  "v_exp_f32 v79, v79\n\t" \
  "v_exp_f32 v80, v80\n\t" \
  "v_exp_f32 v81, v81\n\t" \
  "v_smfmac_f32_32x32x32_bf16 v[96:111], v[144:147], v[72:79], v154\n\t" \
  "s_waitcnt lgkmcnt(3)\n\t" \
  "v_exp_f32 v82, v82\n\t" \
  "v_exp_f32 v83, v83\n\t" \
  "v_exp_f32 v84, v84\n\t" \
  "v_exp_f32 v85, v85\n\t" \
  "v_mfma_f32_32x32x16_bf16 v[64:79], v[156:159], v[128:131], 0\n\t" \
  "v_exp_f32 v86, v86\n\t" \
  "v_exp_f32 v87, v87\n\t" \
  "v_exp_f32 v88, v88\n\t" \
  "v_exp_f32 v89, v89\n\t" \
  "v_smfmac_f32_32x32x32_bf16 v[112:127], v[140:143], v[80:87], v154\n\t" \
  "s_mov_b64 exec, %[msk]\n\t" \
  "ds_read_b128 v[140:143], v152 offset:5120\n\t" \
  "ds_read_b128 v[168:171], v152 offset:5376\n\t" \
  "s_mov_b64 exec, -1\n\t" \
  "v_exp_f32 v90, v90\n\t" \
  "v_exp_f32 v91, v91\n\t" \
  "v_exp_f32 v92, v92\n\t" \
  "v_exp_f32 v93, v93\n\t" \
  "v_exp_f32 v94, v94\n\t" \
  "v_exp_f32 v95, v95\n\t" \
  "v_add_u32 v153, %[cur], v155\n\t" \
  "v_exp_f32 v64, v64\n\t" \
  "v_exp_f32 v65, v65\n\t" \
  "v_smfmac_f32_32x32x32_bf16 v[112:127], v[144:147], v[88:95], v154\n\t" \
  "v_exp_f32 v66, v66\n\t" \
  "v_exp_f32 v67, v67\n\t" \
  "v_exp_f32 v68, v68\n\t" \
  "v_exp_f32 v69, v69\n\t" \
  "v_mfma_f32_32x32x16_bf16 v[80:95], v[156:159], v[132:135], 0\n\t" \
  "s_waitcnt lgkmcnt(3)\n\t" \
  "ds_read_b128 v[156:159], v152 offset:6144\n\t" \
  "v_exp_f32 v70, v70\n\t" \
  "v_exp_f32 v71, v71\n\t" \
  "v_exp_f32 v72, v72\n\t" \
  "v_exp_f32 v73, v73\n\t" \
  "v_smfmac_f32_32x32x32_bf16 v[96:111], v[164:167], v[64:71], v154\n\t" \
  "v_exp_f32 v74, v74\n\t" \
  "v_exp_f32 v75, v75\n\t" \
  "v_exp_f32 v76, v76\n\t" \
  "v_exp_f32 v77, v77\n\t" \
  "v_exp_f32 v78, v78\n\t" \
  "v_exp_f32 v79, v79\n\t" \
  "v_exp_f32 v80, v80\n\t" \
  "v_exp_f32 v81, v81\n\t" \
  "v_smfmac_f32_32x32x32_bf16 v[96:111], v[148:151], v[72:79], v154\n\t" \
  "s_waitcnt lgkmcnt(3)\n\t" \
  "v_exp_f32 v82, v82\n\t" \
  "v_exp_f32 v83, v83\n\t" \
  "v_exp_f32 v84, v84\n\t" \
  "v_exp_f32 v85, v85\n\t" \
  "v_mfma_f32_32x32x16_bf16 v[64:79], v[136:139], v[128:131], 0\n\t" \
  "v_exp_f32 v86, v86\n\t" \
  "v_exp_f32 v87, v87\n\t" \
  "v_exp_f32 v88, v88\n\t" \
  "v_exp_f32 v89, v89\n\t" \
  "v_smfmac_f32_32x32x32_bf16 v[112:127], v[164:167], v[80:87], v154\n\t" \
  "s_mov_b64 exec, %[msk]\n\t" \
  "ds_read_b128 v[164:167], v152 offset:7168\n\t" \
  "ds_read_b128 v[172:175], v152 offset:7424\n\t" \
  "s_mov_b64 exec, -1\n\t" \
  "v_exp_f32 v90, v90\n\t" \
  "v_exp_f32 v91, v91\n\t" \
  "v_exp_f32 v92, v92\n\t" \
  "v_exp_f32 v93, v93\n\t" \
  "v_exp_f32 v94, v94\n\t" \
  "v_exp_f32 v95, v95\n\t" \
  "v_exp_f32 v64, v64\n\t" \
  "v_exp_f32 v65, v65\n\t" \
  "v_smfmac_f32_32x32x32_bf16 v[112:127], v[148:151], v[88:95], v154\n\t" \
  "v_exp_f32 v66, v66\n\t" \
  "v_exp_f32 v67, v67\n\t" \
  "v_exp_f32 v68, v68\n\t" \
  "v_exp_f32 v69, v69\n\t" \
  "v_mfma_f32_32x32x16_bf16 v[80:95], v[136:139], v[132:135], 0\n\t" \
  "s_waitcnt lgkmcnt(3)\n\t" \
  "ds_read_b128 v[136:139], v153 offset:0\n\t" \
  "v_exp_f32 v70, v70\n\t" \
  "v_exp_f32 v71, v71\n\t" \
  "v_exp_f32 v72, v72\n\t" \
  "v_exp_f32 v73, v73\n\t" \
  "v_smfmac_f32_32x32x32_bf16 v[96:111], v[140:143], v[64:71], v154\n\t" \
  "v_exp_f32 v74, v74\n\t" \
  "v_exp_f32 v75, v75\n\t" \
  "v_exp_f32 v76, v76\n\t" \
  "v_exp_f32 v77, v77\n\t" \
  "v_exp_f32 v78, v78\n\t" \
  "v_exp_f32 v79, v79\n\t" \
  "v_exp_f32 v80, v80\n\t" \
  "v_exp_f32 v81, v81\n\t" \
  "v_smfmac_f32_32x32x32_bf16 v[96:111], v[168:171], v[72:79], v154\n\t" \
  "s_waitcnt lgkmcnt(3)\n\t" \
  "v_exp_f32 v82, v82\n\t" \
  "v_exp_f32 v83, v83\n\t" \
  "v_exp_f32 v84, v84\n\t" \
  "v_exp_f32 v85, v85\n\t" \
  "v_mfma_f32_32x32x16_bf16 v[64:79], v[156:159], v[128:131], 0\n\t" \
  "v_exp_f32 v86, v86\n\t" \
  "v_exp_f32 v87, v87\n\t" \
  "v_exp_f32 v88, v88\n\t" \
  "v_exp_f32 v89, v89\n\t" \
  "v_smfmac_f32_32x32x32_bf16 v[112:127], v[140:143], v[80:87], v154\n\t" \
  "s_mov_b64 exec, %[msk]\n\t" \
  "ds_read_b128 v[140:143], v153 offset:1024\n\t" \
  "ds_read_b128 v[144:147], v153 offset:1280\n\t" \
  "s_mov_b64 exec, -1\n\t" \
  "v_exp_f32 v90, v90\n\t" \
  "v_exp_f32 v91, v91\n\t" \
  "v_exp_f32 v92, v92\n\t" \
  "v_exp_f32 v93, v93\n\t" \
  "v_exp_f32 v94, v94\n\t" \
  "v_exp_f32 v95, v95\n\t" \
  "v_exp_f32 v64, v64\n\t" \
  "v_exp_f32 v65, v65\n\t" \
  "v_smfmac_f32_32x32x32_bf16 v[112:127], v[168:171], v[88:95], v154\n\t" \
  "s_cmp_lg_u32 %[n], 0\n\t" \
  "v_exp_f32 v66, v66\n\t" \
  "v_exp_f32 v67, v67\n\t" \
  "v_exp_f32 v68, v68\n\t" \
  "v_exp_f32 v69, v69\n\t" \
  "v_mfma_f32_32x32x16_bf16 v[80:95], v[156:159], v[132:135], 0\n\t" \
  "s_waitcnt lgkmcnt(3)\n\t" \
  "ds_read_b128 v[156:159], v153 offset:2048\n\t" \
  "v_exp_f32 v70, v70\n\t" \
  "v_exp_f32 v71, v71\n\t" \
  "v_exp_f32 v72, v72\n\t" \
  "v_exp_f32 v73, v73\n\t" \
  "v_smfmac_f32_32x32x32_bf16 v[96:111], v[164:167], v[64:71], v154\n\t" \
  "v_exp_f32 v74, v74\n\t" \
  "v_exp_f32 v75, v75\n\t" \
  "v_exp_f32 v76, v76\n\t" \
  "v_exp_f32 v77, v77\n\t" \
  "v_exp_f32 v78, v78\n\t" \
  "v_exp_f32 v79, v79\n\t" \
  "v_exp_f32 v80, v80\n\t" \
  "v_exp_f32 v81, v81\n\t" \
  "v_smfmac_f32_32x32x32_bf16 v[96:111], v[172:175], v[72:79], v154\n\t" \
  "s_waitcnt lgkmcnt(3)\n\t" \
  "v_exp_f32 v82, v82\n\t" \
  "v_exp_f32 v83, v83\n\t" \
  "v_exp_f32 v84, v84\n\t" \
  "v_exp_f32 v85, v85\n\t" \
  "v_mfma_f32_32x32x16_bf16 v[64:79], v[136:139], v[128:131], 0\n\t" \
  "v_exp_f32 v86, v86\n\t" \
  "v_exp_f32 v87, v87\n\t" \
  "v_exp_f32 v88, v88\n\t" \
  "v_exp_f32 v89, v89\n\t" \
  "v_smfmac_f32_32x32x32_bf16 v[112:127], v[164:167], v[80:87], v154\n\t" \
  "s_mov_b64 exec, %[msk]\n\t" \
  "ds_read_b128 v[164:167], v153 offset:3072\n\t" \
  "ds_read_b128 v[148:151], v153 offset:3328\n\t" \
  "s_mov_b64 exec, -1\n\t" \
  "v_exp_f32 v90, v90\n\t" \
  "v_exp_f32 v91, v91\n\t" \
  "v_exp_f32 v92, v92\n\t" \
  "v_exp_f32 v93, v93\n\t" \
  "v_exp_f32 v94, v94\n\t" \
  "v_exp_f32 v95, v95\n\t" \
  "v_mov_b32 v152, v153\n\t" \
  "s_cbranch_scc1 1b\n\t" \
  "s_cmp_eq_u32 %[nn], 0\n\t" \
  "s_cbranch_scc1 9f\n\t" \
  "v_exp_f32 v64, v64\n\t" \
  "v_exp_f32 v65, v65\n\t" \
  "v_smfmac_f32_32x32x32_bf16 v[112:127], v[172:175], v[88:95], v154\n\t" \
  "s_mov_b32 %[cur], %[jmp]\n\t" \
  "s_and_b32 %[n], %[nn], 15\n\t" \
  "v_exp_f32 v66, v66\n\t" \
  "v_exp_f32 v67, v67\n\t" \
  "v_exp_f32 v68, v68\n\t" \
  "v_exp_f32 v69, v69\n\t" \
  "v_mfma_f32_32x32x16_bf16 v[80:95], v[136:139], v[132:135], 0\n\t" \
  "s_waitcnt lgkmcnt(3)\n\t" \
  "ds_read_b128 v[136:139], v152 offset:4096\n\t" \
  "ds_read_b32 v161, v160\n\t" \
  "v_exp_f32 v70, v70\n\t" \
  "v_exp_f32 v71, v71\n\t" \
  "v_exp_f32 v72, v72\n\t" \
  "v_exp_f32 v73, v73\n\t" \
  "v_smfmac_f32_32x32x32_bf16 v[96:111], v[140:143], v[64:71], v154\n\t" \
  "s_lshr_b32 %[nn], %[nn], 4\n\t" \
  "s_add_u32 %[jmp], %[jmp], 0x8000\n\t" \
  "v_exp_f32 v74, v74\n\t" \
  "v_exp_f32 v75, v75\n\t" \
  "v_exp_f32 v76, v76\n\t" \
  "v_exp_f32 v77, v77\n\t" \
  "v_exp_f32 v78, v78\n\t" \
  "v_exp_f32 v79, v79\n\t" \
  "v_exp_f32 v80, v80\n\t" \
  "v_exp_f32 v81, v81\n\t" \
  "v_smfmac_f32_32x32x32_bf16 v[96:111], v[144:147], v[72:79], v154\n\t" \
  "s_waitcnt lgkmcnt(4)\n\t" \
  "v_exp_f32 v82, v82\n\t" \
  "v_exp_f32 v83, v83\n\t" \
  "v_exp_f32 v84, v84\n\t" \
  "v_exp_f32 v85, v85\n\t" \
  "v_mfma_f32_32x32x16_bf16 v[64:79], v[156:159], v[128:131], 0\n\t" \
  "v_exp_f32 v86, v86\n\t" \
  "v_exp_f32 v87, v87\n\t" \
  "v_exp_f32 v88, v88\n\t" \
  "v_exp_f32 v89, v89\n\t" \
  "v_smfmac_f32_32x32x32_bf16 v[112:127], v[140:143], v[80:87], v154\n\t" \
  "s_mov_b64 exec, %[msk]\n\t" \
  "ds_read_b128 v[140:143], v152 offset:5120\n\t" \
  "ds_read_b128 v[168:171], v152 offset:5376\n\t" \
  "s_mov_b64 exec, -1\n\t" \
  "v_exp_f32 v90, v90\n\t" \
  "v_exp_f32 v91, v91\n\t" \
  "v_exp_f32 v92, v92\n\t" \
  "v_exp_f32 v93, v93\n\t" \
  "v_exp_f32 v94, v94\n\t" \
  "v_exp_f32 v95, v95\n\t" \
  "v_add_u32 v153, %[cur], v155\n\t" \
  "v_exp_f32 v64, v64\n\t" \
  "v_exp_f32 v65, v65\n\t" \
  "v_smfmac_f32_32x32x32_bf16 v[112:127], v[144:147], v[88:95], v154\n\t" \
  "v_exp_f32 v66, v66\n\t" \
  "v_exp_f32 v67, v67\n\t" \
  "v_exp_f32 v68, v68\n\t" \
  "v_exp_f32 v69, v69\n\t" \
  "v_mfma_f32_32x32x16_bf16 v[80:95], v[156:159], v[132:135], 0\n\t" \
  "s_waitcnt lgkmcnt(2)\n\t" \
  "ds_read_b128 v[156:159], v152 offset:6144\n\t" \
  "v_exp_f32 v70, v70\n\t" \
  "v_exp_f32 v71, v71\n\t" \
  "v_exp_f32 v72, v72\n\t" \
  "v_exp_f32 v73, v73\n\t" \
  "v_smfmac_f32_32x32x32_bf16 v[96:111], v[164:167], v[64:71], v154\n\t" \
  "v_readfirstlane_b32 %[tmp], v161\n\t" \
  "v_exp_f32 v74, v74\n\t" \
  "v_exp_f32 v75, v75\n\t" \
  "v_exp_f32 v76, v76\n\t" \
  "v_exp_f32 v77, v77\n\t" \
  "v_exp_f32 v78, v78\n\t" \
  "v_exp_f32 v79, v79\n\t" \
  "v_exp_f32 v80, v80\n\t" \
  "v_exp_f32 v81, v81\n\t" \
  "v_smfmac_f32_32x32x32_bf16 v[96:111], v[148:151], v[72:79], v154\n\t" \
  "s_waitcnt lgkmcnt(3)\n\t" \
  "s_cmp_ge_u32 %[tmp], 4\n\t" \
  "v_exp_f32 v82, v82\n\t" \
  "v_exp_f32 v83, v83\n\t" \
  "v_exp_f32 v84, v84\n\t" \
  "v_exp_f32 v85, v85\n\t" \
  "v_mfma_f32_32x32x16_bf16 v[64:79], v[136:139], v[128:131], 0\n\t" \
  "s_cbranch_scc0 7f\n\t" \
  "6:\n\t" \
  "v_exp_f32 v86, v86\n\t" \
  "v_exp_f32 v87, v87\n\t" \
  "v_exp_f32 v88, v88\n\t" \
  "v_exp_f32 v89, v89\n\t" \
  "v_smfmac_f32_32x32x32_bf16 v[112:127], v[164:167], v[80:87], v154\n\t" \
  "s_mov_b64 exec, %[msk]\n\t" \
  "ds_read_b128 v[164:167], v152 offset:7168\n\t" \
  "ds_read_b128 v[172:175], v152 offset:7424\n\t" \
  "s_mov_b64 exec, -1\n\t" \
  "v_exp_f32 v90, v90\n\t" \
  "v_exp_f32 v91, v91\n\t" \
  "v_exp_f32 v92, v92\n\t" \
  "v_exp_f32 v93, v93\n\t" \
  "v_exp_f32 v94, v94\n\t" \
  "v_exp_f32 v95, v95\n\t" \
  "v_exp_f32 v64, v64\n\t" \
  "v_exp_f32 v65, v65\n\t" \
  "v_smfmac_f32_32x32x32_bf16 v[112:127], v[148:151], v[88:95], v154\n\t" \
  "v_exp_f32 v66, v66\n\t" \
  "v_exp_f32 v67, v67\n\t" \
  "v_exp_f32 v68, v68\n\t" \
  "v_exp_f32 v69, v69\n\t" \
  "v_mfma_f32_32x32x16_bf16 v[80:95], v[136:139], v[132:135], 0\n\t" \
  "s_waitcnt lgkmcnt(3)\n\t" \
  "ds_read_b128 v[136:139], v153 offset:0\n\t" \
  "v_exp_f32 v70, v70\n\t" \
  "v_exp_f32 v71, v71\n\t" \
  "v_exp_f32 v72, v72\n\t" \
  "v_exp_f32 v73, v73\n\t" \
  "v_smfmac_f32_32x32x32_bf16 v[96:111], v[140:143], v[64:71], v154\n\t" \
  "v_exp_f32 v74, v74\n\t" \
  "v_exp_f32 v75, v75\n\t" \
  "v_exp_f32 v76, v76\n\t" \
  "v_exp_f32 v77, v77\n\t" \
  "v_exp_f32 v78, v78\n\t" \
  "v_exp_f32 v79, v79\n\t" \
  "v_exp_f32 v80, v80\n\t" \
  "v_exp_f32 v81, v81\n\t" \
  "v_smfmac_f32_32x32x32_bf16 v[96:111], v[168:171], v[72:79], v154\n\t" \
  "s_waitcnt lgkmcnt(3)\n\t" \
  "v_exp_f32 v82, v82\n\t" \
  "v_exp_f32 v83, v83\n\t" \
  "v_exp_f32 v84, v84\n\t" \
  "v_exp_f32 v85, v85\n\t" \
  "v_mfma_f32_32x32x16_bf16 v[64:79], v[156:159], v[128:131], 0\n\t" \
  "v_exp_f32 v86, v86\n\t" \
  "v_exp_f32 v87, v87\n\t" \
  "v_exp_f32 v88, v88\n\t" \
  "v_exp_f32 v89, v89\n\t" \
  "v_smfmac_f32_32x32x32_bf16 v[112:127], v[140:143], v[80:87], v154\n\t" \
  "s_mov_b64 exec, %[msk]\n\t" \
  "ds_read_b128 v[140:143], v153 offset:1024\n\t" \
  "ds_read_b128 v[144:147], v153 offset:1280\n\t" \
  "s_mov_b64 exec, -1\n\t" \
  "v_exp_f32 v90, v90\n\t" \
  "v_exp_f32 v91, v91\n\t" \
  "v_exp_f32 v92, v92\n\t" \
  "v_exp_f32 v93, v93\n\t" \
  "v_exp_f32 v94, v94\n\t" \
  "v_exp_f32 v95, v95\n\t" \
  "v_exp_f32 v64, v64\n\t" \
  "v_exp_f32 v65, v65\n\t" \
  "v_smfmac_f32_32x32x32_bf16 v[112:127], v[168:171], v[88:95], v154\n\t" \
  "v_exp_f32 v66, v66\n\t" \
  "v_exp_f32 v67, v67\n\t" \
  "v_exp_f32 v68, v68\n\t" \
  "v_exp_f32 v69, v69\n\t" \
  "v_mfma_f32_32x32x16_bf16 v[80:95], v[156:159], v[132:135], 0\n\t" \
  "s_waitcnt lgkmcnt(3)\n\t" \
  "ds_read_b128 v[156:159], v153 offset:2048\n\t" \
  "v_exp_f32 v70, v70\n\t" \
  "v_exp_f32 v71, v71\n\t" \
  "v_exp_f32 v72, v72\n\t" \
  "v_exp_f32 v73, v73\n\t" \
  "v_smfmac_f32_32x32x32_bf16 v[96:111], v[164:167], v[64:71], v154\n\t" \
  "v_exp_f32 v74, v74\n\t" \
  "v_exp_f32 v75, v75\n\t" \
  "v_exp_f32 v76, v76\n\t" \
  "v_exp_f32 v77, v77\n\t" \
  "v_exp_f32 v78, v78\n\t" \
  "v_exp_f32 v79, v79\n\t" \
  "v_exp_f32 v80, v80\n\t" \
  "v_exp_f32 v81, v81\n\t" \
  "v_smfmac_f32_32x32x32_bf16 v[96:111], v[172:175], v[72:79], v154\n\t" \
  "s_waitcnt lgkmcnt(3)\n\t" \
  "v_exp_f32 v82, v82\n\t" \
  "v_exp_f32 v83, v83\n\t" \
  "v_exp_f32 v84, v84\n\t" \
  "v_exp_f32 v85, v85\n\t" \
  "v_mfma_f32_32x32x16_bf16 v[64:79], v[136:139], v[128:131], 0\n\t" \
  "v_exp_f32 v86, v86\n\t" \
  "v_exp_f32 v87, v87\n\t" \
  "v_exp_f32 v88, v88\n\t" \
  "v_exp_f32 v89, v89\n\t" \
  "v_smfmac_f32_32x32x32_bf16 v[112:127], v[164:167], v[80:87], v154\n\t" \
  "s_mov_b64 exec, %[msk]\n\t" \
  "ds_read_b128 v[164:167], v153 offset:3072\n\t" \
  "ds_read_b128 v[148:151], v153 offset:3328\n\t" \
  "ds_add_u32 v160, v162 offset:12\n\t" \
  "s_mov_b64 exec, -1\n\t" \
  "v_exp_f32 v90, v90\n\t" \
  "v_exp_f32 v91, v91\n\t" \
  "v_exp_f32 v92, v92\n\t" \
  "v_exp_f32 v93, v93\n\t" \
  "v_exp_f32 v94, v94\n\t" \
  "v_exp_f32 v95, v95\n\t" \
  "v_mov_b32 v152, v153\n\t" \
  "v_add_u32 v160, 4, v160\n\t" \
  "s_branch 1b\n\t" \
  "7:\n\t" \
  "s_mov_b32 %[spin], 0x400000\n\t" \
  "8:\n\t" \
  "s_sleep 1\n\t" \
  "ds_read_b32 v161, v160\n\t" \
  "s_waitcnt lgkmcnt(0)\n\t" \
  "v_readfirstlane_b32 %[tmp], v161\n\t" \
  "s_cmp_ge_u32 %[tmp], 4\n\t" \
  "s_cbranch_scc1 6b\n\t" \
  "s_sub_u32 %[spin], %[spin], 1\n\t" \
  "s_cbranch_scc0 8b\n\t" \
  "s_branch 6b\n\t" \
  "9:\n\t" \
  "s_waitcnt lgkmcnt(0)\n\t" \
  "s_nop 4\n\t"

#define OUT_PROJ \
  "buffer_load_dwordx4 v[0:3], %[os], %[rs], %[o0] offen offset:0 sc1\n\t" \
  "buffer_load_dwordx4 v[4:7], %[os], %[rs], %[o0] offen offset:1024 sc1\n\t" \
  "buffer_load_dwordx4 v[8:11], %[os], %[rs], %[o0] offen offset:2048 sc1\n\t" \
  "buffer_load_dwordx4 v[12:15], %[os], %[rs], %[o0] offen offset:3072 sc1\n\t" \
  "buffer_load_dwordx4 v[16:19], %[os], %[rs], %[o1] offen offset:0 sc1\n\t" \
  "buffer_load_dwordx4 v[20:23], %[os], %[rs], %[o1] offen offset:1024 sc1\n\t" \
  "buffer_load_dwordx4 v[24:27], %[os], %[rs], %[o1] offen offset:2048 sc1\n\t" \
  "buffer_load_dwordx4 v[28:31], %[os], %[rs], %[o1] offen offset:3072 sc1\n\t" \
  "s_mov_b32 %[n], 0x10000\n\t" \
  "s_waitcnt vmcnt(6)\n\t" \
  "1:\n\t" \
  "v_max3_u32 %[t], v0, v1, v2\n\t" \
  "v_max3_u32 %[t], %[t], v3, v4\n\t" \
  "v_max3_u32 %[t], %[t], v5, v6\n\t" \
  "v_max_u32 %[t], %[t], v7\n\t" \
  "v_cmp_eq_u32 vcc, -1, %[t]\n\t" \
  "s_nop 1\n\t" \
  "s_cbranch_vccz 5f\n\t" \
  "s_sub_u32 %[n], %[n], 1\n\t" \
  "s_cbranch_scc1 5f\n\t" \
  "s_waitcnt vmcnt(0)\n\t" \
  "buffer_load_dwordx4 v[0:3], %[os], %[rs], %[o0] offen offset:0 sc1\n\t" \
  "buffer_load_dwordx4 v[4:7], %[os], %[rs], %[o0] offen offset:1024 sc1\n\t" \
  "v_max3_u32 %[t], v8, v9, v10\n\t" \
  "v_max3_u32 %[t], %[t], v11, v12\n\t" \
  "v_max3_u32 %[t], %[t], v13, v14\n\t" \
  "v_max_u32 %[t], %[t], v15\n\t" \
  "v_cmp_eq_u32 vcc, -1, %[t]\n\t" \
  "s_nop 1\n\t" \
  "s_cbranch_vccz 9f\n\t" \
  "buffer_load_dwordx4 v[8:11], %[os], %[rs], %[o0] offen offset:2048 sc1\n\t" \
  "buffer_load_dwordx4 v[12:15], %[os], %[rs], %[o0] offen offset:3072 sc1\n\t" \
  "9:\n\t" \
  "v_max3_u32 %[t], v16, v17, v18\n\t" \
  "v_max3_u32 %[t], %[t], v19, v20\n\t" \
  "v_max3_u32 %[t], %[t], v21, v22\n\t" \
  "v_max_u32 %[t], %[t], v23\n\t" \
  "v_cmp_eq_u32 vcc, -1, %[t]\n\t" \
  "s_nop 1\n\t" \
  "s_cbranch_vccz 9f\n\t" \
  "buffer_load_dwordx4 v[16:19], %[os], %[rs], %[o1] offen offset:0 sc1\n\t" \
  "buffer_load_dwordx4 v[20:23], %[os], %[rs], %[o1] offen offset:1024 sc1\n\t" \
  "9:\n\t" \
  "v_max3_u32 %[t], v24, v25, v26\n\t" \
  "v_max3_u32 %[t], %[t], v27, v28\n\t" \
  "v_max3_u32 %[t], %[t], v29, v30\n\t" \
  "v_max_u32 %[t], %[t], v31\n\t" \
  "v_cmp_eq_u32 vcc, -1, %[t]\n\t" \
  "s_nop 1\n\t" \
  "s_cbranch_vccz 9f\n\t" \
  "buffer_load_dwordx4 v[24:27], %[os], %[rs], %[o1] offen offset:2048 sc1\n\t" \
  "buffer_load_dwordx4 v[28:31], %[os], %[rs], %[o1] offen offset:3072 sc1\n\t" \
  "9:\n\t" \
  "s_waitcnt vmcnt(0)\n\t" \
  "s_branch 1b\n\t" \
  "5:\n\t" \
  "v_mfma_f32_32x32x16_bf16 v[48:63], v[0:3], %[b0], 0\n\t" \
  "v_mfma_f32_32x32x16_bf16 v[48:63], v[4:7], %[b1], v[48:63]\n\t" \
  "v_mfma_f32_32x32x16_bf16 v[32:47], v[0:3], %[a0], 0\n\t" \
  "v_mfma_f32_32x32x16_bf16 v[32:47], v[4:7], %[a1], v[32:47]\n\t" \
  "s_mov_b32 %[n], 0x10000\n\t" \
  "s_waitcnt vmcnt(4)\n\t" \
  "2:\n\t" \
  "v_max3_u32 %[t], v8, v9, v10\n\t" \
  "v_max3_u32 %[t], %[t], v11, v12\n\t" \
  "v_max3_u32 %[t], %[t], v13, v14\n\t" \
  "v_max_u32 %[t], %[t], v15\n\t" \
  "v_cmp_eq_u32 vcc, -1, %[t]\n\t" \
  "s_nop 1\n\t" \
  "s_cbranch_vccz 6f\n\t" \
  "s_sub_u32 %[n], %[n], 1\n\t" \
  "s_cbranch_scc1 6f\n\t" \
  "s_waitcnt vmcnt(0)\n\t" \
  "buffer_load_dwordx4 v[8:11], %[os], %[rs], %[o0] offen offset:2048 sc1\n\t" \
  "buffer_load_dwordx4 v[12:15], %[os], %[rs], %[o0] offen offset:3072 sc1\n\t" \
  "v_max3_u32 %[t], v16, v17, v18\n\t" \
  "v_max3_u32 %[t], %[t], v19, v20\n\t" \
  "v_max3_u32 %[t], %[t], v21, v22\n\t" \
  "v_max_u32 %[t], %[t], v23\n\t" \
  "v_cmp_eq_u32 vcc, -1, %[t]\n\t" \
  "s_nop 1\n\t" \
  "s_cbranch_vccz 9f\n\t" \
  "buffer_load_dwordx4 v[16:19], %[os], %[rs], %[o1] offen offset:0 sc1\n\t" \
  "buffer_load_dwordx4 v[20:23], %[os], %[rs], %[o1] offen offset:1024 sc1\n\t" \
  "9:\n\t" \
  "v_max3_u32 %[t], v24, v25, v26\n\t" \
  "v_max3_u32 %[t], %[t], v27, v28\n\t" \
  "v_max3_u32 %[t], %[t], v29, v30\n\t" \
  "v_max_u32 %[t], %[t], v31\n\t" \
  "v_cmp_eq_u32 vcc, -1, %[t]\n\t" \
  "s_nop 1\n\t" \
  "s_cbranch_vccz 9f\n\t" \
  "buffer_load_dwordx4 v[24:27], %[os], %[rs], %[o1] offen offset:2048 sc1\n\t" \
  "buffer_load_dwordx4 v[28:31], %[os], %[rs], %[o1] offen offset:3072 sc1\n\t" \
  "9:\n\t" \
  "s_waitcnt vmcnt(0)\n\t" \
  "s_branch 2b\n\t" \
  "6:\n\t" \
  "v_mfma_f32_32x32x16_bf16 v[48:63], v[8:11], %[b2], v[48:63]\n\t" \
  "v_mfma_f32_32x32x16_bf16 v[48:63], v[12:15], %[b3], v[48:63]\n\t" \
  "v_mfma_f32_32x32x16_bf16 v[32:47], v[8:11], %[a2], v[32:47]\n\t" \
  "v_mfma_f32_32x32x16_bf16 v[32:47], v[12:15], %[a3], v[32:47]\n\t" \
  "s_mov_b32 %[n], 0x10000\n\t" \
  "s_waitcnt vmcnt(2)\n\t" \
  "3:\n\t" \
  "v_max3_u32 %[t], v16, v17, v18\n\t" \
  "v_max3_u32 %[t], %[t], v19, v20\n\t" \
  "v_max3_u32 %[t], %[t], v21, v22\n\t" \
  "v_max_u32 %[t], %[t], v23\n\t" \
  "v_cmp_eq_u32 vcc, -1, %[t]\n\t" \
  "s_nop 1\n\t" \
  "s_cbranch_vccz 7f\n\t" \
  "s_sub_u32 %[n], %[n], 1\n\t" \
  "s_cbranch_scc1 7f\n\t" \
  "s_waitcnt vmcnt(0)\n\t" \
  "buffer_load_dwordx4 v[16:19], %[os], %[rs], %[o1] offen offset:0 sc1\n\t" \
  "buffer_load_dwordx4 v[20:23], %[os], %[rs], %[o1] offen offset:1024 sc1\n\t" \
  "v_max3_u32 %[t], v24, v25, v26\n\t" \
  "v_max3_u32 %[t], %[t], v27, v28\n\t" \
  "v_max3_u32 %[t], %[t], v29, v30\n\t" \
  "v_max_u32 %[t], %[t], v31\n\t" \
  "v_cmp_eq_u32 vcc, -1, %[t]\n\t" \
  "s_nop 1\n\t" \
  "s_cbranch_vccz 9f\n\t" \
  "buffer_load_dwordx4 v[24:27], %[os], %[rs], %[o1] offen offset:2048 sc1\n\t" \
  "buffer_load_dwordx4 v[28:31], %[os], %[rs], %[o1] offen offset:3072 sc1\n\t" \
  "9:\n\t" \
  "s_waitcnt vmcnt(0)\n\t" \
  "s_branch 3b\n\t" \
  "7:\n\t" \
  "v_mfma_f32_32x32x16_bf16 v[48:63], v[16:19], %[b4], v[48:63]\n\t" \
  "v_mfma_f32_32x32x16_bf16 v[48:63], v[20:23], %[b5], v[48:63]\n\t" \
  "v_mfma_f32_32x32x16_bf16 v[32:47], v[16:19], %[a4], v[32:47]\n\t" \
  "v_mfma_f32_32x32x16_bf16 v[32:47], v[20:23], %[a5], v[32:47]\n\t" \
  "s_mov_b32 %[n], 0x10000\n\t" \
  "s_waitcnt vmcnt(0)\n\t" \
  "4:\n\t" \
  "v_max3_u32 %[t], v24, v25, v26\n\t" \
  "v_max3_u32 %[t], %[t], v27, v28\n\t" \
  "v_max3_u32 %[t], %[t], v29, v30\n\t" \
  "v_max_u32 %[t], %[t], v31\n\t" \
  "v_cmp_eq_u32 vcc, -1, %[t]\n\t" \
  "s_nop 1\n\t" \
  "s_cbranch_vccz 8f\n\t" \
  "s_sub_u32 %[n], %[n], 1\n\t" \
  "s_cbranch_scc1 8f\n\t" \
  "s_waitcnt vmcnt(0)\n\t" \
  "buffer_load_dwordx4 v[24:27], %[os], %[rs], %[o1] offen offset:2048 sc1\n\t" \
  "buffer_load_dwordx4 v[28:31], %[os], %[rs], %[o1] offen offset:3072 sc1\n\t" \
  "s_waitcnt vmcnt(0)\n\t" \
  "s_branch 4b\n\t" \
  "8:\n\t" \
  "v_mfma_f32_32x32x16_bf16 v[48:63], v[24:27], %[b6], v[48:63]\n\t" \
  "v_mfma_f32_32x32x16_bf16 v[48:63], v[28:31], %[b7], v[48:63]\n\t" \
  "v_mfma_f32_32x32x16_bf16 v[32:47], v[24:27], %[a6], v[32:47]\n\t" \
  "v_mfma_f32_32x32x16_bf16 v[32:47], v[28:31], %[a7], v[32:47]\n\t" \
  "s_nop 15\n\t" \
  "ds_write_b128 %[e3], v[56:59]\n\t" \
  "ds_write_b128 %[e3], v[60:63] offset:1024\n\t" \
  "ds_write_b128 %[e2], v[48:51]\n\t" \
  "ds_write_b128 %[e2], v[52:55] offset:1024\n\t" \
  "s_nop 15\n\t" \
  "ds_write_b128 %[e1], v[40:43]\n\t" \
  "ds_write_b128 %[e1], v[44:47] offset:1024\n\t"

constexpr int g2_ops(int j) { return (j + HAHEAD < 16 ? 2 : 0) + (j == 0 || j == 4 ? 8 : 0); }
constexpr int g2_after_h(int ks) { int n = ks < HAHEAD ? 2 * (HAHEAD - 1 - ks) : 0; for (int j = ks < HAHEAD ? 0 : ks - HAHEAD + 1; j < ks; j++) n += g2_ops(j); return n; }
constexpr int g2_after_c(int c) { const int j0 = (c - 2) * 4; int n = j0 + HAHEAD < 16; for (int j = j0 + 1; j <= 4 * c - 2; j++) n += g2_ops(j); return n; }

__global__ void __launch_bounds__(512, 2) k_fused(Params p) {
  const int g = blockIdx.x, tid = threadIdx.x, l = tid & 63, w = __builtin_amdgcn_readfirstlane(tid >> 6);
  const int rb = (g & 7) + 8 * (g >> 6), hg = (g >> 3) & 7;
  const int tc = ((g & 7) >> 1) * 4 + ((g >> 3) & 3), cg = (g & 1) * 8 + (g >> 5);
  const int h = l >> 5, G = l >> 4, r32 = l & 31;
  const __amdgpu_buffer_rsrc_t wsr = __builtin_amdgcn_make_buffer_rsrc(p.ws, 0, WS_SPAN, 0x00020000);
  const __amdgpu_buffer_rsrc_t outr = __builtin_amdgcn_make_buffer_rsrc(p.out + (size_t)rb * 64 * 1024 + hg * 64, 0, 64 * 4096, 0x00020000);
  const int kb = hg & 3, isv = hg >> 2;
  const u16* xbt = p.xb + (size_t)rb * 64 * 512;
  unsigned vsm = L_SM, vone = 1, lmr, lmi, lmd;
  asm volatile("" : "+v"(vsm), "+v"(vone));
  const unsigned lane16 = l * 16;
  if (w == 4) {
    PRIO(3)
    if (PSK) asm volatile("" :: "s"(xbt), "s"(p.wk), "s"(p.wv));
#pragma unroll
    for (int i = 0; i < 16; i += 4) if (WF) dma4((isv ? p.wv : p.wk) + i * 512, lane16, lane16, lane16, lane16, L_WKV + i * 1024);
#pragma unroll
    for (int i = 0; i < 16; i += 4) {
      dma4(xbt + (kb * 16 + i) * 512, (l ^ i) * 16, (l ^ (i + 1)) * 16, (l ^ (i + 2)) * 16, (l ^ (i + 3)) * 16, L_XB + (kb * 16 + i) * 1024);
      if (!WF) dma4((isv ? p.wv : p.wk) + i * 512, lane16, lane16, lane16, lane16, L_WKV + i * 1024);
      if (i == PDLY) bar();
    }
    if (l < 32) st_dev64(p.ws + WS_E + cg * 4096 + (15 - tc) * 256 + l * 8, ~0ull);
  } else {
    if (w == 7 && l < 16) *(LAS unsigned*)(uintptr_t)(L_SM + l * 4) = 0;
    if (w == CPW) {
      unsigned co = CPF == 1 ? ((g >> 3) * 16 + (l & 15)) * 128 : (((g >> 3) & 7) * 64 + l) * 128;
      asm volatile("s_getpc_b64 s[92:93]\nk_code_a:\n\ts_and_b32 s93, s93, 0xffff\n\ts_mov_b32 s94, k_code_z - k_code_a\n\ts_mov_b32 s95, 0x20000\n\ts_nop 0\n\tbuffer_load_dword %0, s[92:95], 0 offen lds"
                   :: "v"(co), "{m0}"(L_DUMMY) : "s92", "s93", "s94", "s95", "scc", "memory");
    }
    bar();
    if (w < 3) {
      const int r0 = (w + (w >= kb)) * 16;
#pragma unroll
      for (int i = 0; i < 16; i += 4) dma4(xbt + (r0 + i) * 512, (l ^ i) * 16, (l ^ (i + 1)) * 16, (l ^ (i + 2)) * 16, (l ^ (i + 3)) * 16, L_XB + (r0 + i) * 1024);
    } else {
      if (w == 6) asm volatile("global_load_ushort %0, %3, %4\n\tglobal_load_ushort %1, %3, %5\n\tglobal_load_ushort %2, %3, %6"
                               : "=&v"(lmr), "=&v"(lmi), "=&v"(lmd) : "v"(r32 * 2), "s"(p.lre + cg * 32), "s"(p.lim + cg * 32), "s"(p.ldt + cg * 32) : "memory");
      const unsigned vo = (l >> 3) * 1024 + (((l & 7) ^ (((l >> 4) & 1) << 2)) * 16);
      const int p0 = (w == 3 ? 0 : w - 4) * 16;
#pragma unroll
      for (int i = 0; i < 16; i += 4) { const int q = p0 + ((i + ((ROT & 1) ? (g >> 6) * 4 : 0)) & 15); dma4(p.wq + q * 8 * 512 + hg * 64, vo, vo + 7168, vo + 14336, vo + 21504, L_WQ + q * 1024); }
      if (PFX && (w == 5 || w == 7)) {
        const u16* tb = w == 5 ? p.xa + (size_t)(tc * 128 + (g >> 5) * 16) * 512 : ((g & 16) ? p.bim : p.bre) + (size_t)(cg * 32 + (g & 8) * 2) * 512;
        touch(tb, (l >> 2) * 1024 + (l & 3) * 128);
        if (PFX == 2) touch(tb + 256, (l >> 2) * 1024 + (l & 3) * 128);
      }
    }
  }
  u64 tag;
  asm volatile("s_load_dwordx2 %0, %1, 0x0\n\ts_waitcnt lgkmcnt(0)" : "=s"(tag) : "s"(p.ws + WS_EPOCH) : "memory");
  tag += 1;
  f32x16 o0, o1;
  if (w == 4) {
    const int key = l & 15, r = kb * 16 + key;
    lp xa[4];
#pragma unroll
    for (int i = 0; i < 4; i++) xa[i] = LP(L_XB + r * 1024 + (((4 * i + G) ^ key) * 16));
    const lp wa = LP(L_WKV + (8 * G + (key >> 2)) * 32 + (l & 3) * 8);
    const int kt = rb * 2 + (kb >> 1), hf = kb & 1;
    unsigned off = isv ? 1024 + (hf + 2 * (G >> 1)) * 256 + key * 16 + (G & 1) * 8 : (hf * 16 + key + 32 * (G & 1)) * 16 + (G >> 1) * 8;
    asm volatile("" : "+v"(off));
    bf16x8 xf[16], wf[16];
    if (WF) {
      WAIT_VM(17);
#pragma unroll
      for (int s = 0; s < 16; s++) wf[s] = tr2(wa, s * 1024, s * 1024 + 128);
      FENCE;
    }
    WAIT_VM(1);
    y_addn(Y_RDYQ);
#pragma unroll
    for (int s = 0; s < 16; s++) { xf[s] = AT(bf16x8, xa[s & 3], (s >> 2) * 256); if (!WF) wf[s] = tr2(wa, s * 1024, s * 1024 + 128); }
    f32x4 a0 = {0, 0, 0, 0}, a1 = a0;
    if (isv) {
#pragma unroll
      for (int s = 0; s < 16; s += 2) { a0 = mfma16(xf[s], wf[s], a0); a1 = mfma16(xf[s + 1], wf[s + 1], a1); }
    } else {
#pragma unroll
      for (int s = 0; s < 16; s += 2) { a0 = mfma16(wf[s], xf[s], a0); a1 = mfma16(wf[s + 1], xf[s + 1], a1); }
    }
    a0 += a1;
    __builtin_amdgcn_raw_buffer_store_b64((u32x2){pk2(a0[0], a0[1]), pk2(a0[2], a0[3])}, wsr, off, WS_KV + kt * 2048, 16);
    WAIT_VM(0);
    if (l < KVREP) st_dev64(p.ws + WS_KVF + l * 2048 + (rb * 8 + hg) * 8, tag);
    PRIO(0)
  } else {
    if (w == 6) {
      asm volatile("s_waitcnt vmcnt(16)" : "+v"(lmr), "+v"(lmi), "+v"(lmd));
      const float ar = bf2f(lmr), ai = bf2f(lmi), dt = (float)(__bf16)expf(bf2f(lmd));
      float e = expf(dt * ar), sn, cs; sincosf(dt * ai, &sn, &cs);
      const float lr = e * cs, li = e * sn, nr = lr - 1.f, inv = 2.f / (ar * ar + ai * ai);
      const lp lt = LP(L_LAM + r32 * 16);
      AT(f32x4, lt, 0) = (f32x4){lr, li, (nr * ar + li * ai) * inv, (li * ar - nr * ai) * inv};
      float pr = lr, pi = li; csq(pr, pi); csq(pr, pi); csq(pr, pi);
      float qr = pr, qi = pi; csq(qr, qi);
      AT(f32x4, lt, 512) = (f32x4){pr, pi, qr, qi};
      csq(qr, qi); pr = qr; pi = qi; csq(pr, pi); csq(pr, pi);
      AT(f32x4, lt, 1024) = (f32x4){qr, qi, pr, pi};
      csq(pr, pi);
      AT(f32x2, lt, 1536) = (f32x2){pr, pi};
    }
    if (PFX && (w == 5 || w == 7)) WAIT_VMX(PFX); else WAIT_VM(0);
    y_add(Y_RDYQ);
  }
  if (w < 4) {
    const int pt = w & 1, hp = w >> 1, q = (l & 15) >> 2, pp = l & 3;
    const int pos = pt * 32 + r32;
    lp xa[8];
#pragma unroll
    for (int i = 0; i < 8; i++) { xa[i] = LP(L_XB + pos * 1024 + (((2 * i + h) ^ (pos & 15)) * 16)); asm volatile("" : "+v"(xa[i])); }
    const int col = hp * 32 + (G & 1) * 16 + 4 * pp;
    lp wa = LP(L_WQ + (8 * h + q) * 128 + (((col >> 3) ^ (((q >> 1) & 1) << 2)) * 16) + (col & 4) * 2);
    asm volatile("" : "+v"(wa));
    y_wait(Y_RDYQ, 8);
    f32x16 acc = {0};
    {
      bf16x8 fa[6], fb[6];
#pragma unroll
      for (int s = -5; s < 32; s++) {
        if (s >= 0) acc = mfma32(fa[s % 6], fb[s % 6], acc);
        FENCE;
        const int n = s + 5;
        if (n < 32) { fa[n % 6] = tr2(wa, n * 2048, n * 2048 + 512); fb[n % 6] = AT(bf16x8, xa[n & 7], (n >> 3) * 256); }
        FENCE;
        if (s == 29) y_add(Y_FREEQ);
      }
    }
    constexpr float QS = 0.25f * 1.4426950408889634f;
    u32x4 q0 = {pk2(acc[0] * QS, acc[1] * QS), pk2(acc[2] * QS, acc[3] * QS), pk2(acc[4] * QS, acc[5] * QS), pk2(acc[6] * QS, acc[7] * QS)};
    u32x4 q1 = {pk2(acc[8] * QS, acc[9] * QS), pk2(acc[10] * QS, acc[11] * QS), pk2(acc[12] * QS, acc[13] * QS), pk2(acc[14] * QS, acc[15] * QS)};
    const unsigned one = (r32 == 16 || r32 == 20) ? 0x3F803F80u : 0u;
    const u32x4 vconst = {one, one, one, one};
    u32x4 vl0 = vconst, vl1 = vconst, vh0 = vconst, vh1 = vconst, vh2 = vconst, vh3 = vconst;
    f32x16 X, Y = {0};
    o0 = Y; o1 = Y;
    const f32x16 zero = {0};
    unsigned idx = 0xDDDD, a = L_SA + lane16, b, cur = L_SA, vy = Y_RDYB, vf;
    asm volatile("" : "+v"(q0), "+v"(q1), "+v"(vl0), "+v"(vl1), "+v"(vh0), "+v"(vh1), "+v"(vh2), "+v"(vh3), "+v"(Y), "+v"(o0), "+v"(o1), "+v"(a), "+v"(vy), "+v"(idx));
    y_waitq(Y_RDYA, 4);
    const lp ap = LP(a);
    u32x4 k0 = AT(u32x4, ap, 0), k1 = AT(u32x4, ap, 2048);
    if ((l & 16) == 0) { vl0 = AT(u32x4, ap, 1024); vh0 = AT(u32x4, ap, 1280); vl1 = AT(u32x4, ap, 3072); vh1 = AT(u32x4, ap, 3328); }
    X = mfma32(__builtin_bit_cast(bf16x8, k0), __builtin_bit_cast(bf16x8, q0), zero);
    {
      unsigned n = 2, nn = 0x533, jmp = L_SB, tmp, spin;
      const u64 msk = 0x0000FFFF0000FFFFull;
#define DBGOPS
      asm volatile("s_nop 7\n\ts_nop 3\n\t" ATT_LOOP
        : "+{v[64:79]}"(X), "+{v[80:95]}"(Y), "+{v[96:111]}"(o0), "+{v[112:127]}"(o1), "+{v[136:139]}"(k0), "+{v[156:159]}"(k1), "+{v[140:143]}"(vl0), "+{v[164:167]}"(vl1),
          "+{v[144:147]}"(vh0), "+{v[148:151]}"(vh1), "+{v[168:171]}"(vh2), "+{v[172:175]}"(vh3), "+{v152}"(a), "=&{v153}"(b), "+{v160}"(vy), "=&{v161}"(vf),
          [cur] "+&s"(cur), [n] "+&s"(n), [nn] "+&s"(nn), [jmp] "+&s"(jmp), [tmp] "=&s"(tmp), [spin] "=&s"(spin) DBGOPS
        : "{v[128:131]}"(q0), "{v[132:135]}"(q1), "{v154}"(idx), "{v155}"(lane16), "{v162}"(vone), [msk] "s"(msk) : "scc", "memory");
    }
    o1 = smf(vh3, __builtin_shufflevector(Y, Y, 8, 9, 10, 11, 12, 13, 14, 15), o1, idx);
    auto norm = [&](const f32x16& o) { const float i = __builtin_amdgcn_rcpf(o[8]); u32x4 f;
#pragma unroll
      for (int j = 0; j < 4; j++) f[j] = min(pk2(o[2 * j] * i, o[2 * j + 1] * i), 0xFFFFFFFEu);
      return f; };
    const unsigned oo = WS_OF + ((rb * 2 + pt) * 32 + hg * 4 + hp * 2) * 1024;
    FENCE;
    u32x4 f0 = norm(o0);
    asm volatile("" : "+v"(f0));
    FENCE;
    if (__builtin_expect(__any(max(f2u(o0[8]) - 0x1F800000u, f2u(o1[8]) - 0x1F800000u) > 0x40000000u), 0)) {
      float m0 = -INFINITY, m1 = -INFINITY;
      const bf16x8 qa = __builtin_bit_cast(bf16x8, q0), qb = __builtin_bit_cast(bf16x8, q1);
#pragma nounroll
      for (int kt = 0; kt < 64; kt++) {
        const bf16x8 k = __builtin_bit_cast(bf16x8, __builtin_amdgcn_raw_buffer_load_b128(wsr, lane16, WS_KV + kt * 2048, 16));
        const f32x16 s0 = mfma32(k, qa, zero), s1 = mfma32(k, qb, zero);
#pragma unroll
        for (int j = 0; j < 16; j++) { m0 = fmaxf(m0, s0[j]); m1 = fmaxf(m1, s1[j]); }
      }
      m0 = fmaxf(m0, __shfl_xor(m0, 32)); m1 = fmaxf(m1, __shfl_xor(m1, 32));
      o0 = zero; o1 = zero;
#pragma nounroll
      for (int kt = 0; kt < 64; kt++) {
        const bf16x8 k = __builtin_bit_cast(bf16x8, __builtin_amdgcn_raw_buffer_load_b128(wsr, lane16, WS_KV + kt * 2048, 16));
        u32x4 v0 = __builtin_amdgcn_raw_buffer_load_b128(wsr, lane16 & ~256u, WS_KV + kt * 2048 + 1024, 16), v1 = __builtin_amdgcn_raw_buffer_load_b128(wsr, lane16 & ~256u, WS_KV + kt * 2048 + 1280, 16);
        if (l & 16) { v0 = vconst; v1 = vconst; }
        const f32x16 s0 = mfma32(k, qa, zero), s1 = mfma32(k, qb, zero);
        f32x8 pa, pb, pc, pd;
#pragma unroll
        for (int j = 0; j < 8; j++) { pa[j] = ex2(s0[j] - m0); pb[j] = ex2(s0[8 + j] - m0); pc[j] = ex2(s1[j] - m1); pd[j] = ex2(s1[8 + j] - m1); }
        o0 = smf(v0, pa, o0, idx); o0 = smf(v1, pb, o0, idx); o1 = smf(v0, pc, o1, idx); o1 = smf(v1, pd, o1, idx);
      }
      __builtin_amdgcn_raw_buffer_store_b128(norm(o0), wsr, lane16, oo, 16);
      __builtin_amdgcn_raw_buffer_store_b128(norm(o1), wsr, lane16, oo + 1024, 16);
      asm volatile("; rows redone" ::: "memory");
    } else {
      __builtin_amdgcn_raw_buffer_store_b128(f0, wsr, lane16, oo, 16);
      FENCE;
      __builtin_amdgcn_raw_buffer_store_b128(norm(o1), wsr, lane16, oo + 1024, 16);
      asm volatile("" ::: "memory");
    }
    y_addn(Y_BEND);
  } else {
    const int a = w - 4;
    const char* kv = p.ws + WS_KV;
    const int i8 = l >> 3;
    const unsigned sw0 = ((l & 7) ^ G) * 16, sw1 = ((l & 7) ^ (4 + G)) * 16;
    unsigned ax0 = (16 * (i8 >> 2) + 8 * (i8 & 1) + ((i8 >> 1) & 1)) * 1024 + sw0, ax1 = ax0 - sw0 + sw1 + 1024, bx0 = i8 * 1024 + sw0, bx1 = i8 * 1024 + sw1 + 7168, bx2 = bx0 + 14336, bx3 = bx1 + 14336;
    const u16* xat = p.xa + (size_t)(tc * 128 + a * 32) * 512;
    const u16* bt = ((a >> 1) ? p.bim : p.bre) + (size_t)(cg * 32 + (a & 1) * 16) * 512;
    const unsigned fr = (r32 >> 1) & 7;
    unsigned so[4];
    lp ga[4], gb[4];
#pragma unroll
    for (int s = 0; s < 4; s++) {
      so[s] = r32 * 128 + (((2 * s + h) ^ fr) * 16);
      ga[s] = LP(L_GX + a * 4096 + so[s]); gb[s] = LP(L_GB + so[s]); asm volatile("" : "+v"(ga[s]), "+v"(gb[s]));
    }
    lp pl = LP(L_LAM + r32 * 16), pse = LP(L_SE + r32 * 8), psa = LP(L_SE + a * 256 + r32 * 8);
    const unsigned zt = L_LEFT + a * 4096;
    lp pz0 = LP(zt + r32 * 64 + (((2 * h) ^ (fr & 3)) * 16)), pz1 = LP(zt + r32 * 64 + (((2 * h + 1) ^ (fr & 3)) * 16));
    lp za[2];
#pragma unroll
    for (int r = 0; r < 2; r++) za[r] = LP(zt + (8 * h + 4 * r + ((l & 15) >> 2)) * 64 + ((((G & 1) * 2 + ((l & 3) >> 1)) ^ (2 * r + ((l & 15) >> 3))) * 16) + (l & 1) * 8);
    const unsigned s0 = a < 3 ? L_LEFT + a * 8192 : L_SA, s1 = L_SB + a * 8192;
    lp gc[2][4];
#pragma unroll
    for (int s = 0; s < 4; s++) { gc[0][s] = LP(s1 + so[s]); gc[1][s] = LP(s0 + so[s]); asm volatile("" : "+v"(gc[0][s]), "+v"(gc[1][s])); }
    unsigned eo = h * 256 + r32 * 8, xo = h * 2048 + r32 * 2, oo = h * 8192 + r32 * 4, wvo = (l >> 3) * 1024 + (((l & 7) ^ (((l >> 4) & 1) << 2)) * 16);
    asm volatile("" : "+v"(ax0), "+v"(ax1), "+v"(bx0), "+v"(bx1), "+v"(bx2), "+v"(bx3), "+v"(pl), "+v"(pse), "+v"(pz0), "+v"(pz1), "+v"(za[0]), "+v"(za[1]), "+v"(eo), "+v"(xo), "+v"(oo), "+v"(wvo));
    unsigned e8 = r32 * 8, d2 = r32 * 2, f8 = (l & 15) * HFS, wt = l * 1024, ct128 = l * 128, xt = (l & 15) * 1024;
    lp e0 = LP(s0 + lane16), e1 = LP(s1 + lane16);
    asm volatile("" : "+v"(psa), "+v"(e8), "+v"(d2), "+v"(f8), "+v"(wt), "+v"(ct128), "+v"(xt), "+v"(e0), "+v"(e1));
    const int rot = (ROT & 2) ? (g >> 5) + 2 * (g >> 3) : 0, rot2 = (ROT & 4) ? g >> 6 : 0;
    auto x_half = [&](int c, int i) { dma2(xat + i * 4 * 512 + ((c + rot) & 7) * 64, ax0, ax1, L_GX + (c % 3) * 16384 + (a * 4 + i * 2) * 1024); };
    auto b_part = [&](int c) { dma2(bt + ((c + rot) & 7) * 64, bx0, bx1, L_GB + ((c + 4) % 6) * 8192 + a * 2048); };
    f32x16 u0 = {0}, u1 = {0};
    asm volatile("" : "+v"(u0), "+v"(u1));
    u32x4 ps = {~0u, ~0u, ~0u, ~0u};
    asm volatile("" : "+v"(ps));
    if (a == 1) {
      const unsigned tl = (unsigned)tag, th = (unsigned)(tag >> 32), fo = WS_KVF + (g & (KVREP - 1)) * 2048;
      _Pragma("nounroll") for (int it = 0; it < SPIN; it++) {
        const u32x4 f0 = __builtin_amdgcn_raw_buffer_load_b128(wsr, lane16, fo, 16), f1 = __builtin_amdgcn_raw_buffer_load_b128(wsr, lane16, fo + 1024, 16);
        if (__all(f0[0] == tl && f0[1] == th && f0[2] == tl && f0[3] == th && f1[0] == tl && f1[1] == th && f1[2] == tl && f1[3] == th)) break;
        asm volatile("" ::: "memory");
      }
      y_add(Y_KVOK);
    } else y_waitq(Y_KVOK, 1);
    dma4_dev(kv + a * 6144, lane16, L_SA + a * 6144); dma2_dev(kv + a * 6144 + 4096, lane16, L_SA + a * 6144 + 4096);
    WAIT_VM(0);
    y_add(Y_RDYA);
    y_wait(Y_FREEQ, 4);
    PRIO(GP)
    if (SBF == 2) { dma4_dev(kv + 24576 + a * 8192, lane16, L_SB + a * 8192); dma4_dev(kv + 28672 + a * 8192, lane16, L_SB + a * 8192 + 4096); }
    x_half(0, 0); x_half(0, 1); b_part(0);
    if (!SBF) { x_half(1, 0); x_half(1, 1); b_part(1); }
    if (SBF != 2) { dma4_dev(kv + 24576 + a * 8192, lane16, L_SB + a * 8192); dma4_dev(kv + 28672 + a * 8192, lane16, L_SB + a * 8192 + 4096); }
    if (SBF) { x_half(1, 0); x_half(1, 1); b_part(1); }
    x_half(2, 0); x_half(2, 1);
    bf16x8 af[2][4], b0[2][4], b1[2][4];
#define MF(acc, x, y) { MPRIO1(1) acc = mfma32(x, y, acc); MPRIO1(0) }
    auto g1_read = [&](int c, int s) { af[c & 1][s] = AT(bf16x8, ga[s], (c % 3) * 16384); b0[c & 1][s] = AT(bf16x8, gb[s], ((c + 4) % 6) * 8192); b1[c & 1][s] = AT(bf16x8, gb[s], ((c + 4) % 6) * 8192 + 4096); };
    WAIT_VMX(SBF == 2 ? 10 : 18);
    if (SBF == 2) y_addn(Y_RDYB);
    y_addn(Y_GRP); y_wait(Y_GRP, 4);
    PRIO(GP)
#pragma unroll
    for (int s = 0; s < 4; s++) g1_read(0, s);
#pragma unroll
    for (int c = 0; c < 8; c++) {
      const int k = c & 1;
      FENCE;
      if (c == 1 && !SBF) { WAIT_VM(12); y_addn(Y_RDYB); }
      if (c < 3) b_part(2 * c + 2);
      if (c == 5 || c == 7) dma2_dev(kv + 57344 + (c - 5) * 8192 + a * 4096, lane16, L_SC + (c - 5) * 8192 + a * 4096);
      FENCE;
      MF(u0, af[k][0], b0[k][0])
      if (c < 3) b_part(2 * c + 3);
      if (c == 5 || c == 7) dma2_dev(kv + 57344 + (c - 5) * 8192 + a * 4096 + 2048, lane16, L_SC + (c - 5) * 8192 + a * 4096 + 2048);
      FENCE;
      MF(u1, af[k][0], b1[k][0])
      if (c < 5) { asm volatile("s_waitcnt lgkmcnt(0)" ::: "memory"); x_half(c + 3, 0); }
      FENCE;
      MF(u0, af[k][1], b0[k][1])
      if (c < 5) x_half(c + 3, 1);
      if (c == 3) {
#pragma unroll
        for (int i = 0; i < 2; i++) __builtin_amdgcn_raw_buffer_store_b128(ps, wsr, lane16, WS_OF + ((rb * 2 + (a & 1)) * 32 + hg * 4 + (a >> 1) * 2 + i) * 1024, 16);
      }
      FENCE;
      MF(u1, af[k][1], b1[k][1])
      if (c == 0) { WAIT_VMX(SBF ? 12 : 20); y_add(Y_GRQ); if (SBF == 1) y_addn(Y_RDYB); } else if (c == 1) { WAIT_VM(12); y_add(Y_GRP); } else if (c == 2) { WAIT_VM(12); y_add(Y_GRQ); }
      else if (c == 3) { WAIT_VM(10); y_add(Y_GRP); } else if (c == 5) y_add(Y_GRQ);
      FENCE;
      MF(u0, af[k][2], b0[k][2]) MF(u1, af[k][2], b1[k][2])
      if (c == 0) y_wait(Y_GRQ, 4); else if (c == 1) y_wait(Y_GRP, 8); else if (c == 3) y_wait(Y_GRQ, 8); else if (c == 4) y_wait(Y_GRP, 12); else if (c == 6) y_wait(Y_GRQ, 12);
      PRIO(GP)
      if (c == 4 || c == 5) WAIT_VM(10); else if (c == 6) WAIT_VM(4);
      if (c < 7) { g1_read(c + 1, 0); g1_read(c + 1, 1); }
      FENCE;
      MF(u0, af[k][3], b0[k][3])
      if (c < 7) { g1_read(c + 1, 2); g1_read(c + 1, 3); }
      FENCE;
      MF(u1, af[k][3], b1[k][3])
    }
    FENCE;
    if (CT) { const int j = g >> 6; touch(((j & 2) ? p.cim : p.cre) + (size_t)(hg * 64 + (j & 1) * 32 + a * 8) * 512, ct128); touch(p.xa + (size_t)(rb * 64 + a * 16) * 512 + hg * 64, xt); }
    const f32x4 lm = AT(f32x4, pl, 0), pw = AT(f32x4, pl, 512), pv = AT(f32x4, pl, 1024);
    PRIO(1)
    const f32x2 lr = {lm[0], lm[0]}, li = {lm[1], lm[1]};
    f32x2 er = {u0[0], u0[1]}, ei = {u1[0], u1[1]};
#pragma unroll
    for (int j = 1; j < 8; j++) {
      const f32x2 tr = pfma(lr, er, (f32x2){u0[2 * j], u0[2 * j + 1]}), ti = pfma(lr, ei, (f32x2){u1[2 * j], u1[2 * j + 1]});
      const f32x2 nr = pfma(-li, ei, tr); ei = pfma(li, er, ti); er = nr;
    }
    float fr0, fi0;
    {
      float tr = er[0], ti = ei[0];
      cmac(pw[0], pw[1], tr, ti, er[1], ei[1]);
      auto sr = __builtin_amdgcn_permlane32_swap(f2u(tr), f2u(tr), false, false), si = __builtin_amdgcn_permlane32_swap(f2u(ti), f2u(ti), false, false);
      fr0 = u2f(sr[0]); fi0 = u2f(si[0]);
      tr = fr0; ti = fi0;
      cmac(pw[2], pw[3], tr, ti, u2f(sr[1]), u2f(si[1]));
      PRIO(0)
      AT(f32x2, psa, 0) = (f32x2){tr, ti};
    }
    y_add(Y_GRP);
    WAIT_VMX(CT ? 2 : 0);
    y_addn(Y_RDYC);
    y_wait(Y_GRP, 16);
    f32x2 se[4];
#pragma unroll
    for (int s = 0; s < 4; s++) se[s] = AT(f32x2, pse, s * 256);
    const f32x2 l32 = {pv[0], pv[1]};
    if (a == 3) {
      asm volatile("s_waitcnt lgkmcnt(0)" : "+v"(se[0]), "+v"(se[1]), "+v"(se[2]), "+v"(se[3]));
      PRIO(1)
      const f32x2 e = cpk(l32, cpk(l32, cpk(l32, se[0], se[1]), se[2]), se[3]);
      const u32x2 eb = {min(f2u(e[0]), 0xFFFFFFFEu), min(f2u(e[1]), 0xFFFFFFFEu)};
      __builtin_amdgcn_raw_buffer_store_b64(eb, wsr, e8, WS_E + cg * 4096 + (15 - tc) * 256, 16);
      PRIO(0)
    }
    if (a == 0) {
      const __amdgpu_buffer_rsrc_t er_ = __builtin_amdgcn_make_buffer_rsrc(p.ws + WS_E + cg * 4096 + (16 - tc) * 256, 0, tc * 256, 0x00020000);
      const f32x2 mu = AT(f32x2, pl, 1536);
      u32x2 ee[8];
      if (ESL) __builtin_amdgcn_s_sleep(ESL);
      _Pragma("nounroll") for (int it = 0; it < SPIN; it++) {
#define ELD(k) "buffer_load_dwordx2 %" #k ", %8, %9, 0 offen offset:512*" #k " sc1\n\t"
        asm volatile(ELD(0) ELD(1) ELD(2) ELD(3) ELD(4) ELD(5) ELD(6) ELD(7) "s_waitcnt vmcnt(0)\n\ts_setprio 1"
                     : "=&v"(ee[0]), "=&v"(ee[1]), "=&v"(ee[2]), "=&v"(ee[3]), "=&v"(ee[4]), "=&v"(ee[5]), "=&v"(ee[6]), "=&v"(ee[7]) : "v"(eo), "s"(er_) : "memory");
        unsigned mx = max(ee[0][0], ee[0][1]);
#pragma unroll
        for (int k = 1; k < 8; k++) mx = max(mx, max(ee[k][0], ee[k][1]));
        if (!__any(mx == ~0u)) break;
        PRIO(0)
      }
      f32x2 c = __builtin_bit_cast(f32x2, ee[7]);
#pragma unroll
      for (int k = 6; k >= 0; k--) c = cpk(mu, c, __builtin_bit_cast(f32x2, ee[k]));
      auto sr = __builtin_amdgcn_permlane32_swap(f2u(c[0]), f2u(c[0]), false, false), si = __builtin_amdgcn_permlane32_swap(f2u(c[1]), f2u(c[1]), false, false);
      float tr = u2f(sr[1]), ti = u2f(si[1]);
      cmac(pv[2], pv[3], tr, ti, u2f(sr[0]), u2f(si[0]));
      PRIO(0)
      AT(f32x2, pse, 1024) = (f32x2){tr, ti};
      y_add(Y_CAR);
    } else {
      if (a != 3) {
#pragma unroll
        for (int i = 0; i < 5; i++) dma4_dev(kv + 90112 + (a - 1) * 20480 + i * 4096, lane16, L_SD + (a - 1) * 20480 + i * 4096);
        WAIT_VM(0);
        y_addn(Y_RDYD); y_addn(Y_RDYD);
      }
      y_waitl(Y_CAR, 1);
    }
    f32x2 c = AT(f32x2, pse, 1024);
    asm volatile("s_waitcnt lgkmcnt(0)" : "+v"(c));
    PRIO(1)
#pragma unroll
    for (int s = 0; s < 3; s++) if (s < a) c = cpk(l32, c, se[s]);
    {
      float cr = c[0], ci = c[1], dr = cr, di = ci;
      cmac(pw[2], pw[3], dr, di, fr0, fi0);
      cr = h ? dr : cr; ci = h ? di : ci;
      dr = cr; di = ci;
      cmac(pw[0], pw[1], dr, di, er[0], ei[0]);
      f32x2 hr = {cr, dr}, hi = {ci, di};
      const f32x2 zr = {lm[2], lm[2]}, zi = {lm[3], lm[3]};
      u32x4 wr[2], wi[2];
#pragma unroll
      for (int j = 0; j < 8; j++) {
        const f32x2 tr = pfma(lr, hr, (f32x2){u0[2 * j], u0[2 * j + 1]}), ti = pfma(lr, hi, (f32x2){u1[2 * j], u1[2 * j + 1]});
        const f32x2 nr = pfma(-li, hi, tr); hi = pfma(li, hr, ti); hr = nr;
        const f32x2 yr = pfma(zr, hr, -zi * hi), yi = pfma(-zr, hi, -zi * hr);
        wr[j >> 2][j & 3] = pk2(yr[0], yr[1]); wi[j >> 2][j & 3] = pk2(yi[0], yi[1]);
      }
      PRIO(0)
      AT(u32x4, pz0, 0) = wr[0]; AT(u32x4, pz1, 0) = wr[1]; AT(u32x4, pz0, 2048) = wi[0]; AT(u32x4, pz1, 2048) = wi[1];
    }
    {
      const unsigned ho = WS_HT + ((tc * 4 + a) * 64 + cg * 2) * 1024;
#pragma unroll
      for (int i = 0; i < 4; i++) {
        s16x4 x = __builtin_amdgcn_ds_read_tr16_b64_v4i16((LAS s16x4*)(za[0] + i * 1024)), y = __builtin_amdgcn_ds_read_tr16_b64_v4i16((LAS s16x4*)(za[1] + i * 1024));
        __builtin_amdgcn_raw_buffer_store_b128(__builtin_bit_cast(u32x4, __builtin_shufflevector(x, y, 0, 1, 2, 3, 4, 5, 6, 7)), wsr, lane16, ho + ((i >> 1) * 32 + (i & 1)) * 1024, 16);
      }
    }
    const u16* ct = ((a >> 1) ? p.cim : p.cre) + (size_t)hg * 64 * 512 + (a & 1) * 256;
    auto c_issue = [&](int c, unsigned sl) {
      dma4(ct + (a >> 1) * 32 * 512 + ((c + rot2) & 3) * 64, bx0, bx1, bx2, bx3, sl); dma4(ct + (1 - (a >> 1)) * 32 * 512 + ((c + rot2) & 3) * 64, bx0, bx1, bx2, bx3, sl + 4096);
    };
    FENCE;
    WAIT_VM(0);
    y_add(Y_GRP); y_wait(Y_GRP, 20);
    if (a == 0) asm volatile("s_mov_b64 exec, 1\n\ts_setprio 1\n\tv_mov_b32 %0, 0\n\tv_mov_b32 v2, %1\n\tv_mov_b32 v3, %2\n\ts_setprio 0\n\tglobal_store_dwordx2 %0, v[2:3], %3 sc1\n\ts_mov_b64 exec, -1"
                             : "=&v"(lmr) : "s"((unsigned)tag), "s"((unsigned)(tag >> 32)), "s"(p.ws + WS_HF + (tc * 16 + cg) * HFS) : "v2", "v3", "memory");
    y_wait(Y_FREEB, 128);
    c_issue(0, s1);
    const int omt = a & 1, ont = a >> 1;
    const __amdgpu_buffer_rsrc_t xar = __builtin_amdgcn_make_buffer_rsrc((void*)(p.xa + (size_t)(rb * 64 + omt * 32) * 512 + hg * 64 + ont * 32), 0, 32 * 1024, 0x00020000);
    const __amdgpu_buffer_rsrc_t dkr = __builtin_amdgcn_make_buffer_rsrc((void*)(p.dsk + hg * 64 + ont * 32), 0, 64, 0x00020000);
    u16 xs[16];
#define TROW(j) (16 * ((j) >> 3) + 8 * ((j) & 1) + 4 * (((j) >> 2) & 1) + (((j) >> 1) & 1))
#pragma unroll
    for (int j = 0; j < 16; j++) xs[j] = __builtin_amdgcn_raw_buffer_load_b16(xar, xo, TROW(j) * 1024, 0);
    const u16 dkb = __builtin_amdgcn_raw_buffer_load_b16(dkr, d2, 0, 0);
    y_wait(Y_FREEA, 128);
    c_issue(1, s0);
    if (a == 1) {
      const char* fp = p.ws + WS_HF + (rb >> 1) * 16 * HFS;
      _Pragma("nounroll") for (int it = 0; it < SPIN; it++) {
        u64 f;
        asm volatile("global_load_dwordx2 %0, %1, %2 sc1\n\ts_waitcnt vmcnt(0)\n\ts_setprio 1" : "=v"(f) : "v"(f8), "s"(fp) : "memory");
        const bool ok = __all(f == tag);
        PRIO(0)
        if (ok) break;
        __builtin_amdgcn_s_sleep(1);
      }
      y_add(Y_HOK);
    } else y_waitl(Y_HOK, 1);
    u32x4 hf[16][2];
    const unsigned hb0 = WS_HT + (rb * 128 + a * 16) * 1024;
#define HLD(s, m) asm volatile("buffer_load_dwordx4 %0, %1, %2, %3 offen offset:%4 sc1" : "=v"(hf[s][m]) : "v"(lane16), "s"(wsr), "s"(hb0 + (((m) ^ omt) * 64 + ((((s) >> 2) + rot2) & 3) * 4) * 1024), "n"(((s) & 3) * 1024) : "memory")
#pragma unroll
    for (int s = 0; s < HAHEAD; s++) { HLD(s, 0); HLD(s, 1); }
    f32x16 y[2][2];
    const f32x16 zero = {0};
    bf16x8 c0[2][4], c1[2][4];
    WAIT_VMX(2 * HAHEAD);
#pragma unroll
    for (int s = 0; s < 4; s++) { c0[0][s] = AT(bf16x8, gc[0][s], 0); c1[0][s] = AT(bf16x8, gc[0][s], 4096); }
#pragma unroll
    for (int c = 0; c < 4; c++) {
#pragma unroll
      for (int s = 0; s < 4; s++) {
        const int ks = c * 4 + s;
        asm volatile("s_waitcnt vmcnt(%2)" : "+v"(hf[ks][0]), "+v"(hf[ks][1]) : "n"(g2_after_h(ks)));
        FENCE;
#pragma unroll
        for (int m = 0; m < 2; m++) {
          const bf16x8 hv = __builtin_bit_cast(bf16x8, hf[ks][m]);
          MPRIO(1) y[m][0] = mfma32(hv, c0[c & 1][s], ks ? y[m][0] : zero); MPRIO(0)
          if (ks + HAHEAD < 16) HLD(ks + HAHEAD, m);
          FENCE;
          MPRIO(1) y[m][1] = mfma32(hv, c1[c & 1][s], ks ? y[m][1] : zero); MPRIO(0)
          if (m == 0 && s == 0 && c < 2) { asm volatile("s_waitcnt lgkmcnt(0)" ::: "memory"); c_issue(c + 2, (c & 1) ? s0 : s1); }
          if (m == 1 && s >= 2 && c < 3) {
            if (s == 2 && c > 0) WAIT_VMX(g2_after_c(c + 1));
#pragma unroll
            for (int t = 2 * (s - 2); t < 2 * (s - 1); t++) { c0[(c + 1) & 1][t] = AT(bf16x8, gc[(c + 1) & 1][t], 0); c1[(c + 1) & 1][t] = AT(bf16x8, gc[(c + 1) & 1][t], 4096); }
          }
          FENCE;
        }
      }
    }
    if (ROT & 8) { touch(p.wo + a * 128 * 512 + hg * 64, wt); touch(p.wo + (a * 128 + 64) * 512 + hg * 64, wt); }
    unsigned wv1, wv2, wv3;
    {
      lp ek[3];
      PRIO(1)
#pragma unroll
      for (int x = 1; x < 4; x++) { const int sa = a ^ x; ek[x - 1] = LP((x < 3 ? (sa < 3 ? L_LEFT + sa * 8192 : L_SA) + (x - 1) * 4096 : L_SB + sa * 8192) + lane16); asm volatile("" : "+v"(ek[x - 1])); }
      wv1 = wvo + 7168; wv2 = wvo + 14336; wv3 = wvo + 21504;
      asm volatile("" : "+v"(wv1), "+v"(wv2), "+v"(wv3));
      PRIO(0)
#pragma unroll
      for (int x = 1; x < 4; x++) {
        const f32x16 v = y[x & 1][x >> 1];
#pragma unroll
        for (int i = 0; i < 4; i++) AT(f32x4, x < 3 ? e0 : e1, (x == 2) * 4096 + i * 1024) = (f32x4){v[4 * i], v[4 * i + 1], v[4 * i + 2], v[4 * i + 3]};
      }
      const f32x16 t = y[0][0];
      y_add(Y_GRP); y_wait(Y_GRP, 24);
      f32x4 rv[3][4];
#pragma unroll
      for (int k = 0; k < 3; k++)
#pragma unroll
        for (int i = 0; i < 4; i++) rv[k][i] = AT(f32x4, ek[k], i * 1024);
      y_add(Y_GRP); y_wait(Y_GRP, 28);
      FENCE;
      PRIO(1)
      const float dk = bf2f(dkb);
#pragma unroll
      for (int j = 0; j < 16; j += 2) {
        if (j < 8) { FENCE; dma4(p.wo + (a * 128 + j * 16) * 512 + hg * 64, wvo, wv1, wv2, wv3, (j < 4 ? s0 : s1) + (j & 2) * 2048); FENCE; }
        f32x2 v = {t[j], t[j + 1]};
#pragma unroll
        for (int k = 0; k < 3; k++) v += (f32x2){rv[k][j >> 2][j & 3], rv[k][j >> 2][(j & 3) + 1]};
        const unsigned sk = pk2(bf2f(xs[j]) * dk, bf2f(xs[j + 1]) * dk);
        v += (f32x2){u2f(sk << 16), u2f(sk & 0xFFFF0000u)};
        __builtin_amdgcn_raw_buffer_store_b32(f2u(v[0]), outr, oo, (omt * 32 + TROW(j)) * 4096 + ont * 128, OUTA_AUX);
        __builtin_amdgcn_raw_buffer_store_b32(f2u(v[1]), outr, oo, (omt * 32 + TROW(j + 1)) * 4096 + ont * 128, OUTA_AUX);
      }
      PRIO(0)
    }
    if (a == 1) {
      PRIO(1)
      const int g2 = (g & ~56) | ((l & 7) << 3), tc2 = ((g2 & 7) >> 1) * 4 + ((g2 >> 3) & 3), cg2 = (g2 & 1) * 8 + (g2 >> 5);
      const char* fp = p.ws + WS_HF + (tc2 * 16 + cg2) * HFS;
      _Pragma("nounroll") for (int it = 0; it < SPIN; it++) {
        if (__all(ld_dev64(fp) == tag)) break;
        __builtin_amdgcn_s_sleep(4);
      }
      if (g == 0 && l == 0) st_dev64(p.ws + WS_EPOCH, tag);
      PRIO(0)
    }
    WAIT_VM(10);
    y_add(Y_OOK);
  }
  {
    const int kq = w & 3, mt = w >> 2, q = (l & 15) >> 2, pp = l & 3;
    const unsigned ob = WS_OF + ((rb * 2 + mt) * 32 + kq * 8) * 1024, os = lane16 ^ ((kq & 1) * 256);
    const unsigned s0 = kq < 3 ? L_LEFT + kq * 8192 : L_SA, s1 = L_SB + kq * 8192;
    lp we[2][2];
#pragma unroll
    for (int n = 0; n < 2; n++) {
      const int col = (n ^ (kq >> 1)) * 32 + (G & 1) * 16 + 4 * pp;
      const unsigned wo = (4 * h + q) * 128 + (((col >> 3) ^ (((q >> 1) & 1) << 2)) * 16) + (col & 4) * 2;
      we[n][0] = LP(s0 + wo); we[n][1] = LP(s1 + wo);
    }
    unsigned ew[3]; lp er[3];
#pragma unroll
    for (int x = 1; x < 4; x++) {
      const int iw = w * 3 + x - 1, ir = (w ^ x) * 3 + x - 1;
      ew[x - 1] = (iw < 16 ? L_SC + iw * 2048 : L_SA + 8192 + (iw - 16) * 2048) + lane16;
      er[x - 1] = LP((ir < 16 ? L_SC + ir * 2048 : L_SA + 8192 + (ir - 16) * 2048) + lane16);
      asm volatile("" : "+v"(er[x - 1]));
    }
    bf16x8 wf[8][2];
    y_wait(Y_OOK, 4);
#pragma unroll
    for (int s = 0; s < 8; s++) { wf[s][0] = tr2(we[0][s >> 2], (s & 3) * 2048, (s & 3) * 2048 + 1024); wf[s][1] = tr2(we[1][s >> 2], (s & 3) * 2048, (s & 3) * 2048 + 1024); }
    y_wait(Y_FREEC, 128);
    if (mt) y_waitl(Y_BEND, BW);
    f32x16 y0, y1, oa, og;
    unsigned tv, spin;
    asm volatile(OUT_PROJ : "=&{v[0:15]}"(oa), "=&{v[16:31]}"(og), "=&{v[32:47]}"(y0), "=&{v[48:63]}"(y1), [t] "=&v"(tv), [n] "=&s"(spin)
                 : [os] "v"(os), [rs] "s"(wsr), [o0] "s"(ob), [o1] "s"(ob + 4096), [e1] "v"(ew[0]), [e2] "v"(ew[1]), [e3] "v"(ew[2]),
                   [a0] "v"(wf[0][0]), [a1] "v"(wf[1][0]), [a2] "v"(wf[2][0]), [a3] "v"(wf[3][0]), [a4] "v"(wf[4][0]), [a5] "v"(wf[5][0]), [a6] "v"(wf[6][0]), [a7] "v"(wf[7][0]),
                   [b0] "v"(wf[0][1]), [b1] "v"(wf[1][1]), [b2] "v"(wf[2][1]), [b3] "v"(wf[3][1]), [b4] "v"(wf[4][1]), [b5] "v"(wf[5][1]), [b6] "v"(wf[6][1]), [b7] "v"(wf[7][1])
                 : "vcc", "scc", "memory");
    f32x4 ta = {y0[0], y0[1], y0[2], y0[3]}, tb = {y0[4], y0[5], y0[6], y0[7]};
    bar();
#pragma unroll
    for (int x = 0; x < 3; x++) { ta += AT(f32x4, er[x], 0); tb += AT(f32x4, er[x], 1024); }
    const float t[8] = {ta[0], ta[1], ta[2], ta[3], tb[0], tb[1], tb[2], tb[3]};
#pragma unroll
    for (int j = 0; j < 8; j++)
      __builtin_amdgcn_raw_buffer_store_b32(f2u(t[j]), outr, h * 16384 + r32 * 4, (mt * 32 + (kq & 1) * 16 + (j & 3) + 8 * (j >> 2)) * 4096 + 2048 + (kq >> 1) * 128, OUTB_AUX);
  }
  asm volatile("k_code_z:");
}

extern "C" void kernel_launch(void* const* d_in, const int* in_sizes, int n_in, void* d_out, int out_size, void* d_ws, size_t ws_size, hipStream_t stream) {
  Params p;
  p.xa = (const u16*)d_in[0]; p.lre = (const u16*)d_in[1]; p.lim = (const u16*)d_in[2]; p.bre = (const u16*)d_in[3]; p.bim = (const u16*)d_in[4];
  p.cre = (const u16*)d_in[5]; p.cim = (const u16*)d_in[6]; p.ldt = (const u16*)d_in[7]; p.dsk = (const u16*)d_in[8]; p.xb = (const u16*)d_in[9];
  p.wq = (const u16*)d_in[10]; p.wk = (const u16*)d_in[11]; p.wv = (const u16*)d_in[12]; p.wo = (const u16*)d_in[13];
  p.out = (float*)d_out; p.ws = (char*)d_ws;

  (void)hipFuncSetAttribute((const void*)k_fused, hipFuncAttributeMaxDynamicSharedMemorySize, 163840);
  k_fused<<<256, 512, 163840, stream>>>(p);
}
```
